```python
import torch

def _softmax(x, axis=-1):
    return torch.softmax(x, dim=axis)
def _gelu(x):
    return torch.nn.functional.gelu(x, approximate="tanh")
def _silu(x):
    return torch.nn.functional.silu(x)
def _sigmoid(x):
    return torch.sigmoid(x)
def _layernorm(x, axis=-1, eps=1e-5):
    m = x.mean(dim=axis, keepdim=True)
    v = x.var(dim=axis, keepdim=True, unbiased=False)
    return (x - m) / torch.sqrt(v + eps)
def _scatter_add(tgt, idx, src, axis=0):
    return tgt.clone().index_add_(axis, idx, src)
def _segment_sum(src, seg, n):
    out = torch.zeros((n,) + src.shape[1:], dtype=src.dtype)
    return out.index_add_(0, seg, src)
def _pad(x, pw):
    flat = tuple(v for pair in reversed(pw) for v in pair)
    return torch.nn.functional.pad(x, flat)

kernel_name = 'standalone__gqa_softmax__decode'

def setup_inputs():

    _w = (lambda a: (lambda t: t.to(torch.bfloat16) if t.is_floating_point() else t)(torch.as_tensor(a)))
    B, Hq, Hkv, Sq, Sk, D = 1, 32, 32, 1, 32768, 128
    return {
        "q": _w((lambda *s: torch.randn(*s, dtype=torch.bfloat16) * 0.8)(B, Hq, Sq, D)),
        "k": _w((lambda *s: torch.randn(*s, dtype=torch.bfloat16) * 0.8)(B, Hkv, Sk, D)),
        "v": _w((lambda *s: torch.randn(*s, dtype=torch.bfloat16) * 0.05)(B, Hkv, Sk, D)),
    }

def reference(q, k, v):
    B, Hq, Sq, D = q.shape
    Hkv = k.shape[1]
    rep = Hq // Hkv
    k = (lambda x, n, axis: torch.repeat_interleave(x, n, dim=axis))(k, rep, axis=1)
    v = (lambda x, n, axis: torch.repeat_interleave(x, n, dim=axis))(v, rep, axis=1)
    scale = D ** -0.5
    scores = torch.einsum("bhqd,bhkd->bhqk", q, k) * scale
    p = _softmax(scores, axis=-1)
    return torch.einsum("bhqk,bhkd->bhqd", p, v)
```

```cpp
#include <hip/hip_runtime.h>
#include <hip/hip_bf16.h>
#include <cstdint>

#define NSPLIT 16
#define UNR 4
constexpr int H = 32, SK = 32768, D = 128;
constexpr int WAVES = 4, NTHR = WAVES * 64;
constexpr int TOK_PER_WG = SK / NSPLIT;
constexpr int TOK_PER_IT = 4 * UNR;
constexpr int NIT = TOK_PER_WG / (WAVES * TOK_PER_IT);

using bf16x2 = __attribute__((ext_vector_type(2))) __bf16;
using u32x4 = __attribute__((ext_vector_type(4))) unsigned;
using f32x4 = __attribute__((ext_vector_type(4))) float;

__device__ __forceinline__ float dot2(unsigned k, unsigned q, float acc) {
  return __builtin_amdgcn_fdot2_f32_bf16(__builtin_bit_cast(bf16x2, k), __builtin_bit_cast(bf16x2, q), acc, false);
}
template <int CTRL> __device__ __forceinline__ float dpp(float x) {
  return __builtin_bit_cast(float, __builtin_amdgcn_mov_dpp(__builtin_bit_cast(int, x), CTRL, 0xf, 0xf, true));
}
constexpr int XOR1 = 0xB1, XOR2 = 0x4E, XOR7 = 0x141, XOR8 = 0x128;
__device__ __forceinline__ float row_sum(float x) {
  x += dpp<XOR1>(x); x += dpp<XOR2>(x); x += dpp<XOR7>(x); x += dpp<XOR8>(x); return x;
}
__device__ __forceinline__ u32x4 ldnt(const void* p) { return __builtin_nontemporal_load(reinterpret_cast<const u32x4*>(p)); }

__global__ void __launch_bounds__(NTHR) attn_split(const uint16_t* __restrict__ q, const uint16_t* __restrict__ k,
                                                   const uint16_t* __restrict__ v, float* __restrict__ ws) {
  __shared__ __attribute__((aligned(16))) float lds[WAVES * 4 * (D + 4)];
  const int s = blockIdx.x % NSPLIT, h = blockIdx.x / NSPLIT;
  const int tid = threadIdx.x, lane = tid & 63, wave = __builtin_amdgcn_readfirstlane(tid >> 6), r = lane >> 4, c = lane & 15;
  constexpr float C2 = 0.088388347648318440f * 1.4426950408889634f;
  const u32x4 qv = *reinterpret_cast<const u32x4*>(q + h * D + 8 * c);
  const size_t base = ((size_t)h * SK + (size_t)s * TOK_PER_WG + (size_t)wave * TOK_PER_IT) * D + lane * 8;
  const uint16_t* kp = k + base;
  const uint16_t* vp = v + base;
  constexpr int STEP = WAVES * TOK_PER_IT * D;
  float m = -INFINITY, l = 0.f, acc[8];
#pragma unroll
  for (int i = 0; i < 8; ++i) acc[i] = 0.f;
  u32x4 KA[UNR], VA[UNR], KB[UNR], VB[UNR];
  auto issue = [&](u32x4* K, u32x4* V, int it) {
#pragma unroll
    for (int t = 0; t < UNR; ++t) K[t] = ldnt(kp + (size_t)it * STEP + t * 512);
#pragma unroll
    for (int t = 0; t < UNR; ++t) V[t] = ldnt(vp + (size_t)it * STEP + t * 512);
  };
  auto compute = [&](const u32x4* K, const u32x4* V) {
    float sc[UNR];
#pragma unroll
    for (int t = 0; t < UNR; ++t) {
      float d = 0.f;
#pragma unroll
      for (int j = 0; j < 4; ++j) d = dot2(K[t][j], qv[j], d);
      sc[t] = row_sum(d) * C2;
    }
    float mn = m;
#pragma unroll
    for (int t = 0; t < UNR; ++t) mn = fmaxf(mn, sc[t]);
    const float alpha = __builtin_amdgcn_exp2f(m - mn);
    m = mn;
    l *= alpha;
#pragma unroll
    for (int i = 0; i < 8; ++i) acc[i] *= alpha;
#pragma unroll
    for (int t = 0; t < UNR; ++t) {
      const float p = __builtin_amdgcn_exp2f(sc[t] - mn);
      l += p;
#pragma unroll
      for (int j = 0; j < 4; ++j) {
        acc[2 * j] = fmaf(p, __builtin_bit_cast(float, V[t][j] << 16), acc[2 * j]);
        acc[2 * j + 1] = fmaf(p, __builtin_bit_cast(float, V[t][j] & 0xffff0000u), acc[2 * j + 1]);
      }
    }
  };
  issue(KA, VA, 0);
#pragma unroll 1
  for (int it = 0; it < NIT; it += 2) {
    issue(KB, VB, it + 1);
    compute(KA, VA);
    if (it + 2 < NIT) issue(KA, VA, it + 2);
    compute(KB, VB);
  }
  float* rec = lds + (wave * 4 + r) * (D + 4);
  *reinterpret_cast<f32x4*>(rec + 4 + 8 * c) = f32x4{acc[0], acc[1], acc[2], acc[3]};
  *reinterpret_cast<f32x4*>(rec + 8 + 8 * c) = f32x4{acc[4], acc[5], acc[6], acc[7]};
  if (c == 0) { rec[0] = m; rec[1] = l; }
  __syncthreads();
  if (tid < D) {
    float M = -INFINITY;
#pragma unroll
    for (int w = 0; w < WAVES * 4; ++w) M = fmaxf(M, lds[w * (D + 4)]);
    float L = 0.f, o = 0.f;
#pragma unroll
    for (int w = 0; w < WAVES * 4; ++w) {
      const float wt = __builtin_amdgcn_exp2f(lds[w * (D + 4)] - M);
      L = fmaf(wt, lds[w * (D + 4) + 1], L);
      o = fmaf(wt, lds[w * (D + 4) + 4 + tid], o);
    }
    float* out = ws + (size_t)blockIdx.x * (D + 4);
    out[4 + tid] = o;
    if (tid == 0) { out[0] = M; out[1] = L; }
  }
}

__global__ void __launch_bounds__(D) attn_combine(const float* __restrict__ ws, __hip_bfloat16* __restrict__ out) {
  const int h = blockIdx.x, tid = threadIdx.x;
  const float* p = ws + (size_t)h * NSPLIT * (D + 4);
  float M = -INFINITY;
#pragma unroll
  for (int s = 0; s < NSPLIT; ++s) M = fmaxf(M, p[s * (D + 4)]);
  float L = 0.f, o = 0.f;
#pragma unroll
  for (int s = 0; s < NSPLIT; ++s) {
    const float wt = __builtin_amdgcn_exp2f(p[s * (D + 4)] - M);
    L = fmaf(wt, p[s * (D + 4) + 1], L);
    o = fmaf(wt, p[s * (D + 4) + 4 + tid], o);
  }
  out[h * D + tid] = __float2bfloat16(o / L);
}

extern "C" void kernel_launch(void* const* d_in, const int* in_sizes, int n_in, void* d_out, int out_size, void* d_ws,
                              size_t ws_size, hipStream_t stream) {
  attn_split<<<H * NSPLIT, NTHR, 0, stream>>>((const uint16_t*)d_in[0], (const uint16_t*)d_in[1], (const uint16_t*)d_in[2], (float*)d_ws);
  attn_combine<<<H, D, 0, stream>>>((const float*)d_ws, (__hip_bfloat16*)d_out);
}
```

```cpp
#include <hip/hip_runtime.h>
#include <hip/hip_bf16.h>
#include <cstdint>

#define CH 4
#define RING 4
constexpr int H = 32, SK = 32768, D = 128, NSPLIT = 8;
constexpr int WAVES = 4, NTHR = WAVES * 64;
constexpr uint32_t TB = CH * 1024;
constexpr uint32_t WG_BYTES = (uint32_t)SK * D * 2 / NSPLIT;
constexpr int NT = WG_BYTES / (TB * WAVES);
static_assert(NT % RING == 0, "");

using bf16x2 = __attribute__((ext_vector_type(2))) __bf16;
using u32x4 = __attribute__((ext_vector_type(4))) unsigned;
using f32x4 = __attribute__((ext_vector_type(4))) float;
using f32x2 = __attribute__((ext_vector_type(2))) float;

__device__ __forceinline__ float dot2(unsigned k, unsigned q, float acc) {
  return __builtin_amdgcn_fdot2_f32_bf16(__builtin_bit_cast(bf16x2, k), __builtin_bit_cast(bf16x2, q), acc, false);
}
template <int CTRL> __device__ __forceinline__ float dpp(float x) {
  return __builtin_bit_cast(float, __builtin_amdgcn_mov_dpp(__builtin_bit_cast(int, x), CTRL, 0xf, 0xf, true));
}
constexpr int XOR1 = 0xB1, XOR2 = 0x4E, XOR7 = 0x141, XOR8 = 0x128;
__device__ __forceinline__ float row_sum(float x) {
  x += dpp<XOR1>(x); x += dpp<XOR2>(x); x += dpp<XOR7>(x); x += dpp<XOR8>(x); return x;
}

__global__ void __launch_bounds__(NTHR, 1) attn_split(const uint16_t* __restrict__ q, const uint16_t* __restrict__ k,
                                                      const uint16_t* __restrict__ v, float* __restrict__ ws) {
  __shared__ __attribute__((aligned(16))) float lds[WAVES * 4 * (D + 4)];
  const int h = blockIdx.x / NSPLIT;
  const int tid = threadIdx.x, lane = tid & 63, wave = __builtin_amdgcn_readfirstlane(tid >> 6), r = lane >> 4, c = lane & 15;
  constexpr float C2 = 0.088388347648318440f * 1.4426950408889634f;
  const auto rk = __builtin_amdgcn_make_buffer_rsrc((void*)k, 0, H * SK * D * 2, 0x00020000);
  const auto rv = __builtin_amdgcn_make_buffer_rsrc((void*)v, 0, H * SK * D * 2, 0x00020000);
  const uint32_t voff = lane * 16;
  uint32_t so = blockIdx.x * WG_BYTES + wave * TB;
  constexpr uint32_t STEP = WAVES * TB;
  u32x4 KB[RING][CH], VB[RING][CH];
  auto issue = [&](u32x4* B, decltype(rk) rs, uint32_t s) {
#pragma unroll
    for (int t = 0; t < CH; ++t) { B[t] = __builtin_amdgcn_raw_buffer_load_b128(rs, voff + (t & 3) * 1024, s + (t >> 2) * 4096, 2); __builtin_amdgcn_sched_barrier(0); }
  };
  const u32x4 qv = *reinterpret_cast<const u32x4*>(q + h * D + 8 * c);
#pragma unroll
  for (int i = 0; i < RING; ++i) { issue(KB[i], rk, so); issue(VB[i], rv, so); so += STEP; }
  float m = -INFINITY, l = 0.f;
  f32x2 acc[4];
#pragma unroll
  for (int i = 0; i < 4; ++i) acc[i] = f32x2{0.f, 0.f};
  float sc[CH];
  auto qk = [&](const u32x4* K) {
#pragma unroll
    for (int t = 0; t < CH; ++t) {
      float d = 0.f;
#pragma unroll
      for (int j = 0; j < 4; ++j) d = dot2(K[t][j], qv[j], d);
      sc[t] = d;
    }
  };
  auto pv = [&](const u32x4* V) {
#pragma unroll
    for (int t = 0; t < CH; ++t) sc[t] = row_sum(sc[t]) * C2;
    float mn = m;
#pragma unroll
    for (int t = 0; t < CH; ++t) mn = fmaxf(mn, sc[t]);
    const float alpha = __builtin_amdgcn_exp2f(m - mn);
    m = mn;
    l *= alpha;
#pragma unroll
    for (int i = 0; i < 4; ++i) acc[i] *= alpha;
#pragma unroll
    for (int t = 0; t < CH; ++t) {
      const float p = __builtin_amdgcn_exp2f(sc[t] - mn);
      l += p;
#pragma unroll
      for (int j = 0; j < 4; ++j) {
        const f32x2 vv = {__builtin_bit_cast(float, V[t][j] << 16), __builtin_bit_cast(float, V[t][j] & 0xffff0000u)};
        acc[j] = __builtin_elementwise_fma(f32x2{p, p}, vv, acc[j]);
      }
    }
  };
  auto pin_sc = [&]() {
#pragma unroll
    for (int t = 0; t < CH; ++t) asm volatile("" : "+v"(sc[t]) :: "memory");
    __builtin_amdgcn_sched_barrier(0);
  };
  auto pin_acc = [&]() {
#pragma unroll
    for (int t = 0; t < 4; ++t) asm volatile("" : "+v"(acc[t]) :: "memory");
    __builtin_amdgcn_sched_barrier(0);
  };
#pragma unroll 1
  for (int it = 0; it < NT - RING; it += RING) {
#pragma unroll
    for (int i = 0; i < RING; ++i) {
      qk(KB[i]); pin_sc();
      issue(KB[i], rk, so);
      __builtin_amdgcn_sched_barrier(0);
      pv(VB[i]); pin_acc();
      issue(VB[i], rv, so);
      __builtin_amdgcn_sched_barrier(0);
      so += STEP;
    }
  }
#pragma unroll
  for (int i = 0; i < RING; ++i) { qk(KB[i]); pv(VB[i]); }
  float* rec = lds + (wave * 4 + r) * (D + 4);
  *reinterpret_cast<f32x4*>(rec + 4 + 8 * c) = f32x4{acc[0][0], acc[0][1], acc[1][0], acc[1][1]};
  *reinterpret_cast<f32x4*>(rec + 8 + 8 * c) = f32x4{acc[2][0], acc[2][1], acc[3][0], acc[3][1]};
  if (c == 0) { rec[0] = m; rec[1] = l; }
  __syncthreads();
  if (tid < D) {
    float M = -INFINITY;
#pragma unroll
    for (int w = 0; w < WAVES * 4; ++w) M = fmaxf(M, lds[w * (D + 4)]);
    float L = 0.f, o = 0.f;
#pragma unroll
    for (int w = 0; w < WAVES * 4; ++w) {
      const float wt = __builtin_amdgcn_exp2f(lds[w * (D + 4)] - M);
      L = fmaf(wt, lds[w * (D + 4) + 1], L);
      o = fmaf(wt, lds[w * (D + 4) + 4 + tid], o);
    }
    float* out = ws + (size_t)blockIdx.x * (D + 4);
    out[4 + tid] = o;
    if (tid == 0) { out[0] = M; out[1] = L; }
  }
}

__global__ void __launch_bounds__(D) attn_combine(const float* __restrict__ ws, __hip_bfloat16* __restrict__ out) {
  const int h = blockIdx.x, tid = threadIdx.x;
  const float* p = ws + (size_t)h * NSPLIT * (D + 4);
  float M = -INFINITY;
#pragma unroll
  for (int s = 0; s < NSPLIT; ++s) M = fmaxf(M, p[s * (D + 4)]);
  float L = 0.f, o = 0.f;
#pragma unroll
  for (int s = 0; s < NSPLIT; ++s) {
    const float wt = __builtin_amdgcn_exp2f(p[s * (D + 4)] - M);
    L = fmaf(wt, p[s * (D + 4) + 1], L);
    o = fmaf(wt, p[s * (D + 4) + 4 + tid], o);
  }
  out[h * D + tid] = __float2bfloat16(o / L);
}

extern "C" void kernel_launch(void* const* d_in, const int* in_sizes, int n_in, void* d_out, int out_size, void* d_ws,
                              size_t ws_size, hipStream_t stream) {
  attn_split<<<H * NSPLIT, NTHR, 0, stream>>>((const uint16_t*)d_in[0], (const uint16_t*)d_in[1], (const uint16_t*)d_in[2], (float*)d_ws);
  attn_combine<<<H, D, 0, stream>>>((const float*)d_ws, (__hip_bfloat16*)d_out);
}
```

```cpp
#define CH 4
#define RING 1
#define STATIC16 12
#define PAT 0
#include <hip/hip_runtime.h>
#include <hip/hip_bf16.h>
#include <cstdint>

#define DYN 1
constexpr int H = 32, SK = 32768, D = 128, NSPLIT = 8;
constexpr int WAVES = 4, NTHR = WAVES * 64;
constexpr uint32_t TB = CH * 1024;
constexpr uint32_t HEAD_BYTES = (uint32_t)SK * D * 2;
constexpr uint32_t KV_BYTES = (uint32_t)H * HEAD_BYTES;
constexpr int NT = HEAD_BYTES / (TB * WAVES * NSPLIT);
constexpr int NS = DYN ? NT * STATIC16 / 16 : NT;
constexpr uint32_t NDYN = (NT - NS) * NSPLIT;
static_assert(NS % RING == 0 && NS >= 2 * RING, "");
constexpr uint32_t WS_EPOCH = 0, WS_CNT = 16384, WS_GRAN = 65536, GRAN_STRIDE = 2048;

using bf16x2 = __attribute__((ext_vector_type(2))) __bf16;
using u32x4 = __attribute__((ext_vector_type(4))) unsigned;
using f32x4 = __attribute__((ext_vector_type(4))) float;
using f32x2 = __attribute__((ext_vector_type(2))) float;
using u64 = unsigned long long;
typedef __attribute__((address_space(1))) unsigned gu32;
typedef __attribute__((address_space(1))) u64 gu64;
#define RLX_AGENT __ATOMIC_RELAXED, __HIP_MEMORY_SCOPE_AGENT

__device__ __forceinline__ float dot2(unsigned k, unsigned q, float acc) {
  return __builtin_amdgcn_fdot2_f32_bf16(__builtin_bit_cast(bf16x2, k), __builtin_bit_cast(bf16x2, q), acc, false);
}
template <int CTRL> __device__ __forceinline__ float dpp(float x) {
  return __builtin_bit_cast(float, __builtin_amdgcn_mov_dpp(__builtin_bit_cast(int, x), CTRL, 0xf, 0xf, true));
}
constexpr int XOR1 = 0xB1, XOR2 = 0x4E, XOR7 = 0x141, XOR8 = 0x128;
__device__ __forceinline__ float row_sum(float x) {
  x += dpp<XOR1>(x); x += dpp<XOR2>(x); x += dpp<XOR7>(x); x += dpp<XOR8>(x); return x;
}
__device__ __forceinline__ float xrow_max(float x) {
  auto s = __builtin_amdgcn_permlane16_swap(__float_as_uint(x), __float_as_uint(x), false, false);
  x = fmaxf(__uint_as_float(s[0]), __uint_as_float(s[1]));
  auto t = __builtin_amdgcn_permlane32_swap(__float_as_uint(x), __float_as_uint(x), false, false);
  return fmaxf(__uint_as_float(t[0]), __uint_as_float(t[1]));
}
__device__ __forceinline__ float xrow_sum(float x) {
  auto s = __builtin_amdgcn_permlane16_swap(__float_as_uint(x), __float_as_uint(x), false, false);
  x = __uint_as_float(s[0]) + __uint_as_float(s[1]);
  auto t = __builtin_amdgcn_permlane32_swap(__float_as_uint(x), __float_as_uint(x), false, false);
  return __uint_as_float(t[0]) + __uint_as_float(t[1]);
}
using i32x4 = __attribute__((ext_vector_type(4))) int;
__device__ long long raw_buffer_atomic_add_i64(long long vdata, i32x4 rsrc, int voffset, int soffset, int aux) __asm("llvm.amdgcn.raw.buffer.atomic.add.i64");
__device__ __forceinline__ unsigned uni(unsigned x) { return __builtin_amdgcn_readfirstlane(x); }
__device__ __forceinline__ float lanef(float x, int l) { return __uint_as_float(__builtin_amdgcn_readlane(__float_as_uint(x), l)); }

__global__ void __launch_bounds__(NTHR, 1) attn_decode(const uint16_t* __restrict__ q, const uint16_t* k,
                                                       const uint16_t* v, __hip_bfloat16* __restrict__ out,
                                                       unsigned char* __restrict__ ws) {
  __shared__ __attribute__((aligned(16))) float lds[WAVES * (D + 4)];
  const int h = blockIdx.x / NSPLIT, s = blockIdx.x % NSPLIT;
  const int tid = threadIdx.x, lane = tid & 63, wave = uni(tid >> 6), r = lane >> 4, c = lane & 15;
  constexpr float C2 = 0.088388347648318440f * 1.4426950408889634f;
  const uint32_t voff = lane * 16;
  uint32_t so = h * HEAD_BYTES + (PAT == 0 ? s * (NS * WAVES * TB) : s * (WAVES * TB)) + wave * TB;
  constexpr uint32_t STEP = PAT == 0 ? WAVES * TB : NSPLIT * WAVES * TB;
  u32x4 KB[RING][CH], VB[RING][CH];
  auto issue = [&](u32x4* B, const uint16_t* base, uint32_t nrec, uint32_t a) {
    const auto rs = __builtin_amdgcn_make_buffer_rsrc((void*)base, 0, nrec, 0x00020000);
#pragma unroll
    for (int t = 0; t < CH; ++t) { B[t] = __builtin_amdgcn_raw_buffer_load_b128(rs, voff + (t & 3) * 1024, a + (t >> 2) * 4096, 2); __builtin_amdgcn_sched_barrier(0); }
  };
  gu32* const epoch_p = (gu32*)(ws + WS_EPOCH + h * 256);
  gu64* const cnt_p = (gu64*)(ws + WS_CNT + (h * WAVES + wave) * 256);
  const unsigned epoch_v = __hip_atomic_load(epoch_p, RLX_AGENT);
  const u32x4 qv = *reinterpret_cast<const u32x4*>(q + h * D + 8 * c);
  __builtin_amdgcn_sched_barrier(0);
#pragma unroll
  for (int i = 0; i < RING; ++i) { issue(KB[i], k, KV_BYTES, so); issue(VB[i], v, KV_BYTES, so); so += STEP; }
  const unsigned epoch = uni(epoch_v);
  const unsigned tag = (epoch ^ 0x5bd1e995u) * 0x9E3779B1u + 0x7f4a7c15u;
  if (DYN && s == 0 && lane == 0) __hip_atomic_store(cnt_p, (u64)tag << 32, RLX_AGENT);
  float m = -INFINITY, l = 0.f;
  f32x2 acc[4];
#pragma unroll
  for (int i = 0; i < 4; ++i) acc[i] = f32x2{0.f, 0.f};
  float sc[CH];
  auto qk = [&](const u32x4* K) {
#pragma unroll
    for (int t = 0; t < CH; ++t) {
      float d = 0.f;
#pragma unroll
      for (int j = 0; j < 4; ++j) d = dot2(K[t][j], qv[j], d);
      sc[t] = d;
    }
#pragma unroll
    for (int t = 0; t < CH; ++t) asm volatile("" : "+v"(sc[t]) :: "memory");
    __builtin_amdgcn_sched_barrier(0);
  };
  auto pv = [&](const u32x4* V) {
#pragma unroll
    for (int t = 0; t < CH; ++t) sc[t] = row_sum(sc[t]) * C2;
    float mn = m;
#pragma unroll
    for (int t = 0; t < CH; ++t) mn = fmaxf(mn, sc[t]);
    const float alpha = __builtin_amdgcn_exp2f(m - mn);
    m = mn;
    l *= alpha;
#pragma unroll
    for (int i = 0; i < 4; ++i) acc[i] *= alpha;
#pragma unroll
    for (int t = 0; t < CH; ++t) {
      const float p = __builtin_amdgcn_exp2f(sc[t] - mn);
      l += p;
#pragma unroll
      for (int j = 0; j < 4; ++j) {
        const f32x2 vv = {__builtin_bit_cast(float, V[t][j] << 16), __builtin_bit_cast(float, V[t][j] & 0xffff0000u)};
        acc[j] = __builtin_elementwise_fma(f32x2{p, p}, vv, acc[j]);
      }
    }
#pragma unroll
    for (int t = 0; t < 4; ++t) asm volatile("" : "+v"(acc[t]) :: "memory");
    __builtin_amdgcn_sched_barrier(0);
  };
#pragma unroll 1
  for (int it = 0; it < NS - (DYN ? 2 : 1) * RING; it += RING) {
#pragma unroll
    for (int i = 0; i < RING; ++i) {
      qk(KB[i]); issue(KB[i], k, KV_BYTES, so);
      pv(VB[i]); issue(VB[i], v, KV_BYTES, so);
      so += STEP;
    }
  }
  if constexpr (!DYN) {
#pragma unroll
    for (int i = 0; i < RING; ++i) { qk(KB[i]); pv(VB[i]); }
  } else {
    u64 tk[RING];
    const u64 cnt_a = (u64)(ws + WS_CNT + (h * WAVES + wave) * 256);
    auto claim = [&](int nrec) {
      const i32x4 rs = {(int)(unsigned)cnt_a, (int)(unsigned)(cnt_a >> 32) & 0xffff, nrec, 0x00020000};
      return (u64)raw_buffer_atomic_add_i64(1ll, rs, (int)voff, 0, 0);
    };
#pragma unroll
    for (int i = 0; i < RING; ++i) {
      qk(KB[i]); tk[i] = claim(8); __builtin_amdgcn_sched_barrier(0); issue(KB[i], k, KV_BYTES, so);
      pv(VB[i]); issue(VB[i], v, KV_BYTES, so);
      so += STEP;
    }
    const uint32_t dbase = h * HEAD_BYTES + NS * NSPLIT * WAVES * TB + wave * TB;
    uint32_t live[RING];
#pragma unroll
    for (int i = 0; i < RING; ++i) live[i] = KV_BYTES;
    bool ok = true;
    do {
#pragma unroll
      for (int i = 0; i < RING; ++i) {
        const bool alive = i == 0 || live[i] != 0;
        if (alive) qk(KB[i]);
        unsigned tl = uni((unsigned)tk[i]), th = uni((unsigned)(tk[i] >> 32));
        if (__builtin_expect(ok && th != tag, 0)) {
          for (int spin = 0; spin < (1 << 20) && th != tag; ++spin) { __builtin_amdgcn_s_sleep(8); const u64 t = claim(8); tl = uni((unsigned)t); th = uni((unsigned)(t >> 32)); }
        }
        ok = ok && tl < NDYN;
        const uint32_t nrec = ok ? KV_BYTES : 0u, a = ok ? dbase + tl * (WAVES * TB) : 0u;
        tk[i] = claim(ok ? 8 : 0); __builtin_amdgcn_sched_barrier(0);
        issue(KB[i], k, nrec, a);
        if (alive) pv(VB[i]);
        issue(VB[i], v, nrec, a);
        live[i] = nrec;
      }
    } while (live[0] != 0);
  }
  {
    const float M = xrow_max(m), wt = __builtin_amdgcn_exp2f(m - M);
    const float L = xrow_sum(l * wt);
    float o[8];
#pragma unroll
    for (int j = 0; j < 4; ++j) { o[2 * j] = xrow_sum(acc[j][0] * wt); o[2 * j + 1] = xrow_sum(acc[j][1] * wt); }
    float* rec = lds + wave * (D + 4);
    if (r == 0) {
      *reinterpret_cast<f32x4*>(rec + 4 + 8 * c) = f32x4{o[0], o[1], o[2], o[3]};
      *reinterpret_cast<f32x4*>(rec + 8 + 8 * c) = f32x4{o[4], o[5], o[6], o[7]};
      if (c == 0) *reinterpret_cast<f32x2*>(rec) = f32x2{M, L};
    }
  }
  __syncthreads();
  if (wave < 2) {
    float M = -INFINITY;
#pragma unroll
    for (int w = 0; w < WAVES; ++w) M = fmaxf(M, lds[w * (D + 4)]);
    float L = 0.f, o = 0.f;
#pragma unroll
    for (int w = 0; w < WAVES; ++w) {
      const float wt = __builtin_amdgcn_exp2f(lds[w * (D + 4)] - M);
      L = fmaf(wt, lds[w * (D + 4) + 1], L);
      o = fmaf(wt, lds[w * (D + 4) + 4 + tid], o);
    }
    gu64* const gran = (gu64*)(ws + WS_GRAN + (size_t)h * NSPLIT * GRAN_STRIDE);
    if (s != NSPLIT - 1) {
      gu64* g = gran + s * (GRAN_STRIDE / 8);
      __hip_atomic_store(g + tid, ((u64)tag << 32) | __float_as_uint(o), RLX_AGENT);
      if (tid < 2) __hip_atomic_store(g + D + tid, ((u64)tag << 32) | __float_as_uint(tid == 0 ? M : L), RLX_AGENT);
    } else {
      u64 ga[NSPLIT - 1], gm;
      const int mi = min(lane, 2 * (NSPLIT - 1) - 1);
      for (int spin = 0; spin < (1 << 22); ++spin) {
        gm = __hip_atomic_load(gran + (mi >> 1) * (GRAN_STRIDE / 8) + D + (mi & 1), RLX_AGENT);
#pragma unroll
        for (int j = 0; j < NSPLIT - 1; ++j) ga[j] = __hip_atomic_load(gran + j * (GRAN_STRIDE / 8) + tid, RLX_AGENT);
        bool ok = (unsigned)(gm >> 32) == tag;
#pragma unroll
        for (int j = 0; j < NSPLIT - 1; ++j) ok &= (unsigned)(ga[j] >> 32) == tag;
        if (__all(ok)) break;
        __builtin_amdgcn_s_sleep(1);
      }
      const float gmf = __uint_as_float((unsigned)gm);
      float MM = M;
#pragma unroll
      for (int j = 0; j < NSPLIT - 1; ++j) MM = fmaxf(MM, lanef(gmf, 2 * j));
      const float w0 = __builtin_amdgcn_exp2f(M - MM);
      L *= w0; o *= w0;
#pragma unroll
      for (int j = 0; j < NSPLIT - 1; ++j) {
        const float wt = __builtin_amdgcn_exp2f(lanef(gmf, 2 * j) - MM);
        L = fmaf(wt, lanef(gmf, 2 * j + 1), L);
        o = fmaf(wt, __uint_as_float((unsigned)ga[j]), o);
      }
      out[h * D + tid] = __float2bfloat16(o / L);
      if (tid == 0) __hip_atomic_store(epoch_p, epoch + 1u, RLX_AGENT);
    }
  }
}

extern "C" void kernel_launch(void* const* d_in, const int* in_sizes, int n_in, void* d_out, int out_size, void* d_ws,
                              size_t ws_size, hipStream_t stream) {
  attn_decode<<<H * NSPLIT, NTHR, 0, stream>>>((const uint16_t*)d_in[0], (const uint16_t*)d_in[1], (const uint16_t*)d_in[2],
                                               (__hip_bfloat16*)d_out, (unsigned char*)d_ws);
}
```

```cpp
#define CH 1
#define INFK 5
#include <hip/hip_runtime.h>
#include <hip/hip_bf16.h>
#include <cstdint>
#include <type_traits>

#define INFV INFK
#define WAVES 4
constexpr int H = 32, SK = 32768, D = 128, NSPLIT = 8;
constexpr int NTHR = WAVES * 64;
constexpr uint32_t HEAD_BYTES = (uint32_t)SK * D * 2;
constexpr uint32_t KV_BYTES = (uint32_t)H * HEAD_BYTES;
constexpr uint32_t WG_BYTES = HEAD_BYTES / NSPLIT;
constexpr uint32_t GSTEP = 16 * 1024 * WAVES;
constexpr int NGRP = WG_BYTES / GSTEP;
static_assert(16 % CH == 0 && INFK < 16 && INFV < 16 && INFV >= INFK, "");
constexpr uint32_t WS_EPOCH = 0, WS_GRAN = 65536, GRAN_STRIDE = 2048;

using bf16x2 = __attribute__((ext_vector_type(2))) __bf16;
using u32x4 = __attribute__((ext_vector_type(4))) unsigned;
using f32x4 = __attribute__((ext_vector_type(4))) float;
using f32x2 = __attribute__((ext_vector_type(2))) float;
using u64 = unsigned long long;
typedef __attribute__((address_space(1))) unsigned gu32;
typedef __attribute__((address_space(1))) u64 gu64;
#define RLX_AGENT __ATOMIC_RELAXED, __HIP_MEMORY_SCOPE_AGENT

__device__ __forceinline__ float dot2(unsigned k, unsigned q, float acc) {
  return __builtin_amdgcn_fdot2_f32_bf16(__builtin_bit_cast(bf16x2, k), __builtin_bit_cast(bf16x2, q), acc, false);
}
template <int CTRL> __device__ __forceinline__ float dpp(float x) {
  return __builtin_bit_cast(float, __builtin_amdgcn_mov_dpp(__builtin_bit_cast(int, x), CTRL, 0xf, 0xf, true));
}
constexpr int XOR1 = 0xB1, XOR2 = 0x4E, XOR7 = 0x141, XOR8 = 0x128, ROWBCAST = 0x150;
__device__ __forceinline__ float xrow_max(float x) {
  auto s = __builtin_amdgcn_permlane16_swap(__float_as_uint(x), __float_as_uint(x), false, false);
  x = fmaxf(__uint_as_float(s[0]), __uint_as_float(s[1]));
  auto t = __builtin_amdgcn_permlane32_swap(__float_as_uint(x), __float_as_uint(x), false, false);
  return fmaxf(__uint_as_float(t[0]), __uint_as_float(t[1]));
}
__device__ __forceinline__ float xrow_sum(float x) {
  auto s = __builtin_amdgcn_permlane16_swap(__float_as_uint(x), __float_as_uint(x), false, false);
  x = __uint_as_float(s[0]) + __uint_as_float(s[1]);
  auto t = __builtin_amdgcn_permlane32_swap(__float_as_uint(x), __float_as_uint(x), false, false);
  return __uint_as_float(t[0]) + __uint_as_float(t[1]);
}
__device__ __forceinline__ unsigned uni(unsigned x) { return __builtin_amdgcn_readfirstlane(x); }
__device__ __forceinline__ float lanef(float x, int l) { return __uint_as_float(__builtin_amdgcn_readlane(__float_as_uint(x), l)); }
template <int I, int N, class F> __device__ __forceinline__ void sfor(F&& f) {
  if constexpr (I < N) { f(std::integral_constant<int, I>{}); sfor<I + 1, N>(f); }
}

__global__ void __launch_bounds__(NTHR, 1) attn_decode(const uint16_t* q, const uint16_t* k, const uint16_t* v,
                                                       __hip_bfloat16* out, unsigned char* ws) {
  __shared__ __attribute__((aligned(16))) float lds[WAVES * NGRP * 64 + WAVES * (D + 4)];
  const int h = blockIdx.x / NSPLIT, s = blockIdx.x % NSPLIT;
  const int tid = threadIdx.x, lane = tid & 63, wave = uni(tid >> 6), r = lane >> 4, c = lane & 15;
  constexpr float C2 = 0.088388347648318440f * 1.4426950408889634f;
  const uint32_t voff = lane * 16;
  const uint32_t base = blockIdx.x * WG_BYTES + wave * (CH * 1024);
  const auto rk = __builtin_amdgcn_make_buffer_rsrc((void*)k, 0, KV_BYTES, 0x00020000);
  const auto rv = __builtin_amdgcn_make_buffer_rsrc((void*)v, 0, KV_BYTES, 0x00020000);
  u32x4 B[16];
  auto ld = [&](decltype(rk) rs, uint32_t a, auto I) {
    constexpr uint32_t i = decltype(I)::value, off = (i / CH) * (WAVES * CH * 1024) + (i % CH) * 1024;
    B[i] = __builtin_amdgcn_raw_buffer_load_b128(rs, voff + (off & 4095), a + (off & ~4095u), 2);
    __builtin_amdgcn_sched_barrier(0);
  };
  auto arrived = [&](const u32x4& x) { asm volatile("" :: "v"(x) : "memory"); __builtin_amdgcn_sched_barrier(0); };
  gu32* const epoch_p = (gu32*)(ws + WS_EPOCH + h * 256);
  const unsigned epoch_v = __hip_atomic_load(epoch_p, RLX_AGENT);
  const u32x4 qv = *reinterpret_cast<const u32x4*>(q + h * D + 8 * c);
  __builtin_amdgcn_sched_barrier(0);
  sfor<0, INFK>([&](auto I) { ld(rk, base, I); });
  float* const sl = lds + wave * (NGRP * 64) + lane;
  float mx = -INFINITY;
  auto kgroup = [&](uint32_t cur, decltype(rk) rn, uint32_t next, int g) {
    float x[16];
    sfor<0, 16>([&](auto I) {
      constexpr int i = I.value;
      arrived(B[i]);
      if constexpr (i + INFK < 16) ld(rk, cur, std::integral_constant<int, (i + INFK) % 16>{});
      else ld(rn, next, std::integral_constant<int, (i + INFK) % 16>{});
      float d = 0.f;
#pragma unroll
      for (int j = 0; j < 4; ++j) d = dot2(B[i][j], qv[j], d);
      asm volatile("" : "+v"(d) :: "memory");
      x[i] = d;
      __builtin_amdgcn_sched_barrier(0);
    });
#define TR_STEP(HALF, CTRL, BIT) _Pragma("unroll") for (int j = 0; j < (HALF); ++j) { \
      const float lo_ = x[j] + dpp<CTRL>(x[j]), hi_ = x[j + (HALF)] + dpp<CTRL>(x[j + (HALF)]); x[j] = (c & (BIT)) ? hi_ : lo_; }
    TR_STEP(8, XOR8, 8) TR_STEP(4, XOR7, 4) TR_STEP(2, XOR2, 2) TR_STEP(1, XOR1, 1)
#undef TR_STEP
    mx = fmaxf(mx, x[0]);
    sl[64 * g] = x[0];
    __builtin_amdgcn_sched_barrier(0);
  };
#pragma unroll 1
  for (int g = 0; g < NGRP - 1; ++g) kgroup(base + g * GSTEP, rk, base + (g + 1) * GSTEP, g);
  kgroup(base + (NGRP - 1) * GSTEP, rv, base, NGRP - 1);
  sfor<INFK, INFV>([&](auto I) { ld(rv, base, I); });
  mx = fmaxf(mx, dpp<XOR1>(mx)); mx = fmaxf(mx, dpp<XOR2>(mx)); mx = fmaxf(mx, dpp<XOR7>(mx)); mx = fmaxf(mx, dpp<XOR8>(mx));
  const float m = xrow_max(mx) * C2;
  float l = 0.f;
  f32x2 acc[4];
#pragma unroll
  for (int i = 0; i < 4; ++i) acc[i] = f32x2{0.f, 0.f};
  auto vgroup = [&](uint32_t cur, int g, auto LAST) {
    const float p = __builtin_amdgcn_exp2f(fmaf(sl[64 * g], C2, -m));
    l += p;
    sfor<0, 16>([&](auto I) {
      constexpr int i = I.value;
      arrived(B[i]);
      if constexpr (i + INFV < 16) ld(rv, cur, std::integral_constant<int, (i + INFV) % 16>{});
      else if constexpr (!decltype(LAST)::value) ld(rv, cur + GSTEP, std::integral_constant<int, (i + INFV) % 16>{});
      const float pb = dpp<ROWBCAST + i>(p);
#pragma unroll
      for (int j = 0; j < 4; ++j) {
        const f32x2 vv = {__builtin_bit_cast(float, B[i][j] << 16), __builtin_bit_cast(float, B[i][j] & 0xffff0000u)};
        acc[j] = __builtin_elementwise_fma(f32x2{pb, pb}, vv, acc[j]);
      }
#pragma unroll
      for (int j = 0; j < 4; ++j) asm volatile("" : "+v"(acc[j]) :: "memory");
      __builtin_amdgcn_sched_barrier(0);
    });
  };
#pragma unroll 1
  for (int g = 0; g < NGRP - 1; ++g) vgroup(base + g * GSTEP, g, std::false_type{});
  vgroup(base + (NGRP - 1) * GSTEP, NGRP - 1, std::true_type{});
  float* const mrg = lds + WAVES * NGRP * 64;
  {
    l += dpp<XOR1>(l); l += dpp<XOR2>(l); l += dpp<XOR7>(l); l += dpp<XOR8>(l);
    const float L = xrow_sum(l);
    float o[8];
#pragma unroll
    for (int j = 0; j < 4; ++j) { o[2 * j] = xrow_sum(acc[j][0]); o[2 * j + 1] = xrow_sum(acc[j][1]); }
    float* rec = mrg + wave * (D + 4);
    if (r == 0) {
      *reinterpret_cast<f32x4*>(rec + 4 + 8 * c) = f32x4{o[0], o[1], o[2], o[3]};
      *reinterpret_cast<f32x4*>(rec + 8 + 8 * c) = f32x4{o[4], o[5], o[6], o[7]};
      if (c == 0) *reinterpret_cast<f32x2*>(rec) = f32x2{m, L};
    }
  }
  __syncthreads();
  if (wave < 2) {
    const unsigned epoch = uni(epoch_v);
    const unsigned tag = (epoch ^ 0x5bd1e995u) * 0x9E3779B1u + 0x7f4a7c15u;
    float M = -INFINITY;
#pragma unroll
    for (int w = 0; w < WAVES; ++w) M = fmaxf(M, mrg[w * (D + 4)]);
    float L = 0.f, o = 0.f;
#pragma unroll
    for (int w = 0; w < WAVES; ++w) {
      const float wt = __builtin_amdgcn_exp2f(mrg[w * (D + 4)] - M);
      L = fmaf(wt, mrg[w * (D + 4) + 1], L);
      o = fmaf(wt, mrg[w * (D + 4) + 4 + tid], o);
    }
    gu64* const gran = (gu64*)(ws + WS_GRAN + (size_t)h * NSPLIT * GRAN_STRIDE);
    if (s != NSPLIT - 1) {
      gu64* g = gran + s * (GRAN_STRIDE / 8);
      __hip_atomic_store(g + tid, ((u64)tag << 32) | __float_as_uint(o), RLX_AGENT);
      if (tid < 2) __hip_atomic_store(g + D + tid, ((u64)tag << 32) | __float_as_uint(tid == 0 ? M : L), RLX_AGENT);
    } else {
      u64 ga[NSPLIT - 1], gm;
      const int mi = min(lane, 2 * (NSPLIT - 1) - 1);
      for (int spin = 0; spin < (1 << 22); ++spin) {
        gm = __hip_atomic_load(gran + (mi >> 1) * (GRAN_STRIDE / 8) + D + (mi & 1), RLX_AGENT);
#pragma unroll
        for (int j = 0; j < NSPLIT - 1; ++j) ga[j] = __hip_atomic_load(gran + j * (GRAN_STRIDE / 8) + tid, RLX_AGENT);
        bool ok = (unsigned)(gm >> 32) == tag;
#pragma unroll
        for (int j = 0; j < NSPLIT - 1; ++j) ok &= (unsigned)(ga[j] >> 32) == tag;
        if (__all(ok)) break;
        __builtin_amdgcn_s_sleep(1);
      }
      const float gmf = __uint_as_float((unsigned)gm);
      float MM = M;
#pragma unroll
      for (int j = 0; j < NSPLIT - 1; ++j) MM = fmaxf(MM, lanef(gmf, 2 * j));
      const float w0 = __builtin_amdgcn_exp2f(M - MM);
      L *= w0; o *= w0;
#pragma unroll
      for (int j = 0; j < NSPLIT - 1; ++j) {
        const float wt = __builtin_amdgcn_exp2f(lanef(gmf, 2 * j) - MM);
        L = fmaf(wt, lanef(gmf, 2 * j + 1), L);
        o = fmaf(wt, __uint_as_float((unsigned)ga[j]), o);
      }
      out[h * D + tid] = __float2bfloat16(o / L);
      if (tid == 0) __hip_atomic_store(epoch_p, epoch + 1u, RLX_AGENT);
    }
  }
}

extern "C" void kernel_launch(void* const* d_in, const int* in_sizes, int n_in, void* d_out, int out_size, void* d_ws,
                              size_t ws_size, hipStream_t stream) {
  attn_decode<<<H * NSPLIT, NTHR, 0, stream>>>((const uint16_t*)d_in[0], (const uint16_t*)d_in[1], (const uint16_t*)d_in[2],
                                               (__hip_bfloat16*)d_out, (unsigned char*)d_ws);
}
```

```cpp
#define CH 1
#define INFK 5
#define DIAG 0
#include <hip/hip_runtime.h>
#include <hip/hip_bf16.h>
#include <cstdint>
#include <type_traits>

#define INFV INFK
#define WAVES 4
constexpr int H = 32, SK = 32768, D = 128, NSPLIT = 8;
constexpr int NTHR = WAVES * 64;
constexpr uint32_t HEAD_BYTES = (uint32_t)SK * D * 2;
constexpr uint32_t KV_BYTES = (uint32_t)H * HEAD_BYTES;
constexpr uint32_t WG_BYTES = HEAD_BYTES / NSPLIT;
constexpr uint32_t GSTEP = 16 * 1024 * WAVES;
constexpr int NGRP = WG_BYTES / GSTEP;
static_assert(16 % CH == 0 && INFK < 16 && INFV < 16 && INFV >= INFK, "");
constexpr uint32_t WS_EPOCH = 0, WS_GRAN = 65536, GRAN_STRIDE = 2048;

using bf16x2 = __attribute__((ext_vector_type(2))) __bf16;
using u32x4 = __attribute__((ext_vector_type(4))) unsigned;
using f32x4 = __attribute__((ext_vector_type(4))) float;
using f32x2 = __attribute__((ext_vector_type(2))) float;
using u64 = unsigned long long;
typedef __attribute__((address_space(1))) unsigned gu32;
typedef __attribute__((address_space(1))) u64 gu64;
#define RLX_AGENT __ATOMIC_RELAXED, __HIP_MEMORY_SCOPE_AGENT

__device__ __forceinline__ float dot2(unsigned k, unsigned q, float acc) {
  return __builtin_amdgcn_fdot2_f32_bf16(__builtin_bit_cast(bf16x2, k), __builtin_bit_cast(bf16x2, q), acc, false);
}
template <int CTRL> __device__ __forceinline__ float dpp(float x) {
  return __builtin_bit_cast(float, __builtin_amdgcn_mov_dpp(__builtin_bit_cast(int, x), CTRL, 0xf, 0xf, true));
}
constexpr int XOR1 = 0xB1, XOR2 = 0x4E, XOR7 = 0x141, XOR8 = 0x128, ROWBCAST = 0x150;
__device__ __forceinline__ float xrow_max(float x) {
  auto s = __builtin_amdgcn_permlane16_swap(__float_as_uint(x), __float_as_uint(x), false, false);
  x = fmaxf(__uint_as_float(s[0]), __uint_as_float(s[1]));
  auto t = __builtin_amdgcn_permlane32_swap(__float_as_uint(x), __float_as_uint(x), false, false);
  return fmaxf(__uint_as_float(t[0]), __uint_as_float(t[1]));
}
__device__ __forceinline__ float xrow_sum(float x) {
  auto s = __builtin_amdgcn_permlane16_swap(__float_as_uint(x), __float_as_uint(x), false, false);
  x = __uint_as_float(s[0]) + __uint_as_float(s[1]);
  auto t = __builtin_amdgcn_permlane32_swap(__float_as_uint(x), __float_as_uint(x), false, false);
  return __uint_as_float(t[0]) + __uint_as_float(t[1]);
}
__device__ __forceinline__ unsigned uni(unsigned x) { return __builtin_amdgcn_readfirstlane(x); }
__device__ __forceinline__ float lanef(float x, int l) { return __uint_as_float(__builtin_amdgcn_readlane(__float_as_uint(x), l)); }
template <int I, int N, class F> __device__ __forceinline__ void sfor(F&& f) {
  if constexpr (I < N) { f(std::integral_constant<int, I>{}); sfor<I + 1, N>(f); }
}

__global__ void __launch_bounds__(NTHR, 1) attn_decode(const uint16_t* q, const uint16_t* k, const uint16_t* v,
                                                       __hip_bfloat16* out, unsigned char* ws) {
  __shared__ __attribute__((aligned(16))) float lds[WAVES * NGRP * 64 + WAVES * (D + 4)];
  u64 d0; asm volatile("s_memrealtime %0\n\ts_waitcnt lgkmcnt(0)" : "=s"(d0)); __builtin_amdgcn_sched_barrier(0);
  const int h = blockIdx.x / NSPLIT, s = blockIdx.x % NSPLIT;
  const int tid = threadIdx.x, lane = tid & 63, wave = uni(tid >> 6), r = lane >> 4, c = lane & 15;
  constexpr float C2 = 0.088388347648318440f * 1.4426950408889634f;
  const uint32_t voff = lane * 16;
  const uint32_t base = blockIdx.x * WG_BYTES + wave * (CH * 1024);
  const auto rk = __builtin_amdgcn_make_buffer_rsrc((void*)k, 0, KV_BYTES, 0x00020000);
  const auto rv = __builtin_amdgcn_make_buffer_rsrc((void*)v, 0, KV_BYTES, 0x00020000);
  u32x4 B[16];
  auto ld = [&](decltype(rk) rs, uint32_t a, auto I) {
    constexpr uint32_t i = decltype(I)::value, off = (i / CH) * (WAVES * CH * 1024) + (i % CH) * 1024;
    B[i] = __builtin_amdgcn_raw_buffer_load_b128(rs, voff + (off & 4095), a + (off & ~4095u), 2);
    __builtin_amdgcn_sched_barrier(0);
  };
  auto arrived = [&](const u32x4& x) { asm volatile("" :: "v"(x) : "memory"); __builtin_amdgcn_sched_barrier(0); };
  gu32* const epoch_p = (gu32*)(ws + WS_EPOCH + h * 256);
  const unsigned epoch_v = __hip_atomic_load(epoch_p, RLX_AGENT);
  const u32x4 qv = *reinterpret_cast<const u32x4*>(q + h * D + 8 * c);
  __builtin_amdgcn_sched_barrier(0);
  sfor<0, INFK>([&](auto I) { ld(rk, base, I); });
  arrived(B[0]);
  const u64 d1 = __builtin_amdgcn_s_memrealtime();
  float* const sl = lds + wave * (NGRP * 64) + lane;
  float mx = -INFINITY;
  auto kgroup = [&](uint32_t cur, decltype(rk) rn, uint32_t next, int g) {
    float x[16];
    sfor<0, 16>([&](auto I) {
      constexpr int i = I.value;
      arrived(B[i]);
      if constexpr (i + INFK < 16) ld(rk, cur, std::integral_constant<int, (i + INFK) % 16>{});
      else ld(rn, next, std::integral_constant<int, (i + INFK) % 16>{});
      float d = 0.f;
#pragma unroll
      for (int j = 0; j < 4; ++j) d = dot2(B[i][j], qv[j], d);
      asm volatile("" : "+v"(d) :: "memory");
      x[i] = d;
      __builtin_amdgcn_sched_barrier(0);
    });
#define TR_STEP(HALF, CTRL, BIT) _Pragma("unroll") for (int j = 0; j < (HALF); ++j) { \
      const float lo_ = x[j] + dpp<CTRL>(x[j]), hi_ = x[j + (HALF)] + dpp<CTRL>(x[j + (HALF)]); x[j] = (c & (BIT)) ? hi_ : lo_; }
    TR_STEP(8, XOR8, 8) TR_STEP(4, XOR7, 4) TR_STEP(2, XOR2, 2) TR_STEP(1, XOR1, 1)
#undef TR_STEP
    mx = fmaxf(mx, x[0]);
    sl[64 * g] = x[0];
    __builtin_amdgcn_sched_barrier(0);
  };
#pragma unroll 1
  for (int g = 0; g < NGRP - 1; ++g) kgroup(base + g * GSTEP, rk, base + (g + 1) * GSTEP, g);
  kgroup(base + (NGRP - 1) * GSTEP, rv, base, NGRP - 1);
  sfor<INFK, INFV>([&](auto I) { ld(rv, base, I); });
  const u64 d2 = __builtin_amdgcn_s_memrealtime();
  mx = fmaxf(mx, dpp<XOR1>(mx)); mx = fmaxf(mx, dpp<XOR2>(mx)); mx = fmaxf(mx, dpp<XOR7>(mx)); mx = fmaxf(mx, dpp<XOR8>(mx));
  const float m = xrow_max(mx) * C2;
  float l = 0.f;
  f32x2 acc[4];
#pragma unroll
  for (int i = 0; i < 4; ++i) acc[i] = f32x2{0.f, 0.f};
  auto vgroup = [&](uint32_t cur, int g, auto LAST) {
    const float p = __builtin_amdgcn_exp2f(fmaf(sl[64 * g], C2, -m));
    l += p;
    sfor<0, 16>([&](auto I) {
      constexpr int i = I.value;
      arrived(B[i]);
      if constexpr (i + INFV < 16) ld(rv, cur, std::integral_constant<int, (i + INFV) % 16>{});
      else if constexpr (!decltype(LAST)::value) ld(rv, cur + GSTEP, std::integral_constant<int, (i + INFV) % 16>{});
      const float pb = dpp<ROWBCAST + i>(p);
#pragma unroll
      for (int j = 0; j < 4; ++j) {
        const f32x2 vv = {__builtin_bit_cast(float, B[i][j] << 16), __builtin_bit_cast(float, B[i][j] & 0xffff0000u)};
        acc[j] = __builtin_elementwise_fma(f32x2{pb, pb}, vv, acc[j]);
      }
#pragma unroll
      for (int j = 0; j < 4; ++j) asm volatile("" : "+v"(acc[j]) :: "memory");
      __builtin_amdgcn_sched_barrier(0);
    });
  };
#pragma unroll 1
  for (int g = 0; g < NGRP - 1; ++g) vgroup(base + g * GSTEP, g, std::false_type{});
  vgroup(base + (NGRP - 1) * GSTEP, NGRP - 1, std::true_type{});
  const u64 d3 = __builtin_amdgcn_s_memrealtime();
  float* const mrg = lds + WAVES * NGRP * 64;
  {
    l += dpp<XOR1>(l); l += dpp<XOR2>(l); l += dpp<XOR7>(l); l += dpp<XOR8>(l);
    const float L = xrow_sum(l);
    float o[8];
#pragma unroll
    for (int j = 0; j < 4; ++j) { o[2 * j] = xrow_sum(acc[j][0]); o[2 * j + 1] = xrow_sum(acc[j][1]); }
    float* rec = mrg + wave * (D + 4);
    if (r == 0) {
      *reinterpret_cast<f32x4*>(rec + 4 + 8 * c) = f32x4{o[0], o[1], o[2], o[3]};
      *reinterpret_cast<f32x4*>(rec + 8 + 8 * c) = f32x4{o[4], o[5], o[6], o[7]};
      if (c == 0) *reinterpret_cast<f32x2*>(rec) = f32x2{m, L};
    }
  }
  __syncthreads();
  if (wave < 2) {
    const unsigned epoch = uni(epoch_v);
    const unsigned tag = (epoch ^ 0x5bd1e995u) * 0x9E3779B1u + 0x7f4a7c15u;
    float M = -INFINITY;
#pragma unroll
    for (int w = 0; w < WAVES; ++w) M = fmaxf(M, mrg[w * (D + 4)]);
    float L = 0.f, o = 0.f;
#pragma unroll
    for (int w = 0; w < WAVES; ++w) {
      const float wt = __builtin_amdgcn_exp2f(mrg[w * (D + 4)] - M);
      L = fmaf(wt, mrg[w * (D + 4) + 1], L);
      o = fmaf(wt, mrg[w * (D + 4) + 4 + tid], o);
    }
    gu64* const gran = (gu64*)(ws + WS_GRAN + (size_t)h * NSPLIT * GRAN_STRIDE);
    if (s != NSPLIT - 1) {
      gu64* g = gran + s * (GRAN_STRIDE / 8);
      __hip_atomic_store(g + tid, ((u64)tag << 32) | __float_as_uint(o), RLX_AGENT);
      if (tid < 2) __hip_atomic_store(g + D + tid, ((u64)tag << 32) | __float_as_uint(tid == 0 ? M : L), RLX_AGENT);
    } else {
      u64 ga[NSPLIT - 1], gm;
      const int mi = min(lane, 2 * (NSPLIT - 1) - 1);
      for (int spin = 0; spin < (1 << 22); ++spin) {
        gm = __hip_atomic_load(gran + (mi >> 1) * (GRAN_STRIDE / 8) + D + (mi & 1), RLX_AGENT);
#pragma unroll
        for (int j = 0; j < NSPLIT - 1; ++j) ga[j] = __hip_atomic_load(gran + j * (GRAN_STRIDE / 8) + tid, RLX_AGENT);
        bool ok = (unsigned)(gm >> 32) == tag;
#pragma unroll
        for (int j = 0; j < NSPLIT - 1; ++j) ok &= (unsigned)(ga[j] >> 32) == tag;
        if (__all(ok)) break;
        __builtin_amdgcn_s_sleep(1);
      }
      const float gmf = __uint_as_float((unsigned)gm);
      float MM = M;
#pragma unroll
      for (int j = 0; j < NSPLIT - 1; ++j) MM = fmaxf(MM, lanef(gmf, 2 * j));
      const float w0 = __builtin_amdgcn_exp2f(M - MM);
      L *= w0; o *= w0;
#pragma unroll
      for (int j = 0; j < NSPLIT - 1; ++j) {
        const float wt = __builtin_amdgcn_exp2f(lanef(gmf, 2 * j) - MM);
        L = fmaf(wt, lanef(gmf, 2 * j + 1), L);
        o = fmaf(wt, __uint_as_float((unsigned)ga[j]), o);
      }
      out[h * D + tid] = __float2bfloat16(o / L);
      if (tid == 0) __hip_atomic_store(epoch_p, epoch + 1u, RLX_AGENT);
    }
  }
  asm volatile("s_waitcnt vmcnt(0)" ::: "memory");
  const u64 d4 = __builtin_amdgcn_s_memrealtime();
  {
    long long qq = DIAG == 0 ? 0 : DIAG == 1 ? (long long)(d1 - d0) : DIAG == 2 ? (long long)(d2 - d0) : DIAG == 3 ? (long long)(d3 - d0)
                 : DIAG == 4 ? (long long)(d4 - d0) : DIAG == 5 ? -(long long)(d3 - d0) : DIAG == 6 ? -(long long)(d1 - d0) : -(long long)(d2 - d0);
    const u64 tend = d0 + (qq < 0 ? 25000 : 15000) + qq;
    for (int i = 0; i < 100000 && __builtin_amdgcn_s_memrealtime() < tend; ++i) __builtin_amdgcn_s_sleep(1);
  }
}

extern "C" void kernel_launch(void* const* d_in, const int* in_sizes, int n_in, void* d_out, int out_size, void* d_ws,
                              size_t ws_size, hipStream_t stream) {
  attn_decode<<<H * NSPLIT, NTHR, 0, stream>>>((const uint16_t*)d_in[0], (const uint16_t*)d_in[1], (const uint16_t*)d_in[2],
                                               (__hip_bfloat16*)d_out, (unsigned char*)d_ws);
}
```

```cpp
#define NPOLL 1
#define SOFF 160
#define SLOPE 2180
#include <hip/hip_runtime.h>
#include <hip/hip_bf16.h>
#include <cstdint>
#include <type_traits>

#define CH 8
#define INF 6
constexpr int H = 32, SK = 32768, D = 128, NSPLIT = 8, WAVES = 4, NTHR = WAVES * 64;
constexpr uint32_t HEAD_BYTES = (uint32_t)SK * D * 2;
constexpr uint32_t KV_BYTES = (uint32_t)H * HEAD_BYTES;
constexpr uint32_t WG_BYTES = HEAD_BYTES / NSPLIT;
constexpr uint32_t GSTEP = 16 * 1024 * WAVES;
constexpr int NGRP = WG_BYTES / GSTEP;
static_assert(16 % CH == 0 && INF < 16, "");
constexpr uint32_t WS_EPOCH = 0, WS_ML = 16384, WS_ACC = 65536, WS_END = WS_ACC + H * NSPLIT * 1024;
constexpr int L_SC = 0, L_ACC = WAVES * NGRP * 64, L_MX = L_ACC + WAVES * D, L_SUM = L_MX + WAVES, L_END = L_SUM + WAVES;

using bf16x2 = __attribute__((ext_vector_type(2))) __bf16;
using u32x2 = __attribute__((ext_vector_type(2))) unsigned;
using u32x4 = __attribute__((ext_vector_type(4))) unsigned;
using f32x4 = __attribute__((ext_vector_type(4))) float;
using f32x2 = __attribute__((ext_vector_type(2))) float;
using u64 = unsigned long long;
constexpr int AUX_NT = 2, AUX_SC1 = 16;
typedef __attribute__((address_space(3))) unsigned lu32;
#define RLX_WG __ATOMIC_RELAXED, __HIP_MEMORY_SCOPE_WORKGROUP

__device__ __forceinline__ float dot2(unsigned k, unsigned q, float acc) {
  return __builtin_amdgcn_fdot2_f32_bf16(__builtin_bit_cast(bf16x2, k), __builtin_bit_cast(bf16x2, q), acc, false);
}
template <int CTRL> __device__ __forceinline__ float dpp(float x) {
  return __builtin_bit_cast(float, __builtin_amdgcn_mov_dpp(__builtin_bit_cast(int, x), CTRL, 0xf, 0xf, true));
}
constexpr int XOR1 = 0xB1, XOR2 = 0x4E, XOR7 = 0x141, XOR8 = 0x128, ROWBCAST = 0x150;
__device__ __forceinline__ float fold32(float a, float b) {
  auto t = __builtin_amdgcn_permlane32_swap(__float_as_uint(a), __float_as_uint(b), false, false);
  return __uint_as_float(t[0]) + __uint_as_float(t[1]);
}
__device__ __forceinline__ float fold16(float a, float b) {
  auto t = __builtin_amdgcn_permlane16_swap(__float_as_uint(a), __float_as_uint(b), false, false);
  return __uint_as_float(t[0]) + __uint_as_float(t[1]);
}
__device__ __forceinline__ float wave_max(float x) {
  x = fmaxf(x, dpp<XOR1>(x)); x = fmaxf(x, dpp<XOR2>(x)); x = fmaxf(x, dpp<XOR7>(x)); x = fmaxf(x, dpp<XOR8>(x));
  auto s = __builtin_amdgcn_permlane16_swap(__float_as_uint(x), __float_as_uint(x), false, false);
  x = fmaxf(__uint_as_float(s[0]), __uint_as_float(s[1]));
  auto t = __builtin_amdgcn_permlane32_swap(__float_as_uint(x), __float_as_uint(x), false, false);
  return fmaxf(__uint_as_float(t[0]), __uint_as_float(t[1]));
}
__device__ __forceinline__ float wave_sum(float x) {
  x += dpp<XOR1>(x); x += dpp<XOR2>(x); x += dpp<XOR7>(x); x += dpp<XOR8>(x);
  x = fold16(x, x);
  return fold32(x, x);
}
__device__ __forceinline__ unsigned uni(unsigned x) { return __builtin_amdgcn_readfirstlane(x); }
__device__ __forceinline__ float lanef(float x, int l) { return __uint_as_float(__builtin_amdgcn_readlane(__float_as_uint(x), l)); }
template <int I, int N, class F> __device__ __forceinline__ void sfor(F&& f) {
  if constexpr (I < N) { f(std::integral_constant<int, I>{}); sfor<I + 1, N>(f); }
}
__device__ __forceinline__ void wg_barrier() {
  asm volatile("s_waitcnt lgkmcnt(0)\n\ts_barrier" ::: "memory");
}

__global__ void __launch_bounds__(NTHR, 1) attn_decode(const uint16_t* q, const uint16_t* k, const uint16_t* v,
                                                       __hip_bfloat16* out, unsigned char* ws) {
  __shared__ __attribute__((aligned(16))) float lds[L_END];
  const unsigned tstart = (unsigned)__builtin_amdgcn_s_memrealtime();
  const int h = blockIdx.x / NSPLIT, s = blockIdx.x % NSPLIT;
  const int tid = threadIdx.x, lane = tid & 63, wave = uni(tid >> 6), r = lane >> 4, c = lane & 15;
  constexpr float C2 = 0.088388347648318440f * 1.4426950408889634f;
  const uint32_t voff = lane * 16;
  const uint32_t base = blockIdx.x * WG_BYTES + wave * (CH * 1024);
  const auto rk = __builtin_amdgcn_make_buffer_rsrc((void*)k, 0, KV_BYTES, 0x00020000);
  const auto rv = __builtin_amdgcn_make_buffer_rsrc((void*)v, 0, KV_BYTES, 0x00020000);
  const auto rw = __builtin_amdgcn_make_buffer_rsrc((void*)ws, 0, WS_END, 0x00020000);
  u32x4 B[16];
  auto ld = [&](decltype(rk) rs, uint32_t a, auto I) {
    constexpr uint32_t i = decltype(I)::value, off = (i / CH) * (WAVES * CH * 1024) + (i % CH) * 1024;
    B[i] = __builtin_amdgcn_raw_buffer_load_b128(rs, voff + (off & 4095), a + (off & ~4095u), AUX_NT);
    __builtin_amdgcn_sched_barrier(0);
  };
  int lag;
  auto steer = [&](int G) { lag = (int)((unsigned)__builtin_amdgcn_s_memrealtime() - tstart) - (SOFF + (G * SLOPE) / 10); };
  auto arrived = [&](const u32x4& x, auto THROTTLE) {
    asm volatile("" :: "v"(x) : "memory");
    if constexpr (decltype(THROTTLE)::value)
      asm volatile("s_cmp_gt_i32 %0, 0\n\ts_cbranch_scc1 1f\n\ts_waitcnt vmcnt(%1)\n1:" :: "s"(lag), "i"(INF - 2) : "scc", "memory");
    __builtin_amdgcn_sched_barrier(0);
  };
  const unsigned epoch_v = __builtin_amdgcn_raw_buffer_load_b32(rw, 0, WS_EPOCH + h * 256, AUX_SC1);
  const u32x4 qv = *reinterpret_cast<const u32x4*>(q + h * D + 8 * c);
  __builtin_amdgcn_sched_barrier(0);
  sfor<0, INF>([&](auto I) { ld(rk, base, I); });
  constexpr unsigned PENDING = 0x7fc0dead;
  lu32* const mx_slots = (lu32*)(lds + L_MX);
  if (lane == 0) __hip_atomic_store(mx_slots + wave, PENDING, RLX_WG);
  wg_barrier();
  float* const sl = lds + L_SC + wave * (NGRP * 64) + lane;
  float mx = -INFINITY;
  auto kgroup = [&](uint32_t cur, decltype(rk) rn, uint32_t next, int g) {
    float x[16];
    steer(g);
    sfor<0, 16>([&](auto I) {
      constexpr int i = I.value;
      arrived(B[i], std::true_type{});
      if constexpr (i + INF < 16) ld(rk, cur, std::integral_constant<int, (i + INF) % 16>{});
      else ld(rn, next, std::integral_constant<int, (i + INF) % 16>{});
      float d = 0.f;
#pragma unroll
      for (int j = 0; j < 4; ++j) d = dot2(B[i][j], qv[j], d);
      asm volatile("" : "+v"(d) :: "memory");
      x[i] = d;
      __builtin_amdgcn_sched_barrier(0);
    });
#define TR_STEP(HALF, CTRL, BIT) _Pragma("unroll") for (int j = 0; j < (HALF); ++j) { \
      const float lo_ = x[j] + dpp<CTRL>(x[j]), hi_ = x[j + (HALF)] + dpp<CTRL>(x[j + (HALF)]); x[j] = (c & (BIT)) ? hi_ : lo_; }
    TR_STEP(8, XOR8, 8) TR_STEP(4, XOR7, 4) TR_STEP(2, XOR2, 2) TR_STEP(1, XOR1, 1)
#undef TR_STEP
    mx = fmaxf(mx, x[0]);
    sl[64 * g] = x[0];
    __builtin_amdgcn_sched_barrier(0);
  };
#pragma unroll 1
  for (int g = 0; g < NGRP - 1; ++g) kgroup(base + g * GSTEP, rk, base + (g + 1) * GSTEP, g);
  kgroup(base + (NGRP - 1) * GSTEP, rv, base, NGRP - 1);
  const float m = wave_max(mx) * C2;
  if (lane == 0) __hip_atomic_store(mx_slots + wave, __float_as_uint(m), RLX_WG);
  const unsigned epoch = uni(epoch_v);
  const unsigned tag = (epoch ^ 0x5bd1e995u) * 0x9E3779B1u + 0x7f4a7c15u;
  float mwg, fw;
  float l = 0.f;
  f32x2 acc[4];
#pragma unroll
  for (int i = 0; i < 4; ++i) acc[i] = f32x2{0.f, 0.f};
  auto vgroup = [&](uint32_t cur, int g, auto LAST) {
    constexpr bool last = decltype(LAST)::value;
    steer(NGRP + g);
    const float p = __builtin_amdgcn_exp2f(fmaf(sl[64 * g], C2, -m));
    l += p;
    sfor<0, 16>([&](auto I) {
      constexpr int i = I.value;
      arrived(B[i], std::integral_constant<bool, !last || i + INF < 16>{});
      if constexpr (i + INF < 16) ld(rv, cur, std::integral_constant<int, (i + INF) % 16>{});
      else if constexpr (!last) ld(rv, cur + GSTEP, std::integral_constant<int, (i + INF) % 16>{});
      if constexpr (last && i == 15 - INF) {
        unsigned u[WAVES];
        for (int spin = 0; spin < (1 << 20); ++spin) {
#pragma unroll
          for (int w = 0; w < WAVES; ++w) u[w] = __hip_atomic_load(mx_slots + w, RLX_WG);
          if (uni((u[0] == PENDING) | (u[1] == PENDING) | (u[2] == PENDING) | (u[3] == PENDING)) == 0) break;
        }
        mwg = fmaxf(fmaxf(__uint_as_float(u[0]), __uint_as_float(u[1])), fmaxf(__uint_as_float(u[2]), __uint_as_float(u[3])));
        fw = __builtin_amdgcn_exp2f(m - mwg);
        l = wave_sum(l) * fw;
        if (lane == 0) lds[L_SUM + wave] = l;
        __builtin_amdgcn_raw_buffer_store_b64(u32x2{__float_as_uint(mwg), tag}, rw, tid == 0 ? 0u : WS_END, WS_ML + h * 128 + s * 8, AUX_SC1);
        __builtin_amdgcn_sched_barrier(0);
      }
      const float pb = dpp<ROWBCAST + i>(p);
#pragma unroll
      for (int j = 0; j < 4; ++j) {
        const f32x2 vv = {__builtin_bit_cast(float, B[i][j] << 16), __builtin_bit_cast(float, B[i][j] & 0xffff0000u)};
        acc[j] = __builtin_elementwise_fma(f32x2{pb, pb}, vv, acc[j]);
      }
#pragma unroll
      for (int j = 0; j < 4; ++j) asm volatile("" : "+v"(acc[j]) :: "memory");
      __builtin_amdgcn_sched_barrier(0);
    });
  };
#pragma unroll 1
  for (int g = 0; g < NGRP - 1; ++g) vgroup(base + g * GSTEP, g, std::false_type{});
  vgroup(base + (NGRP - 1) * GSTEP, NGRP - 1, std::true_type{});
  {
    const float y0 = fold32(acc[0][0], acc[2][0]), y1 = fold32(acc[0][1], acc[2][1]);
    const float y2 = fold32(acc[1][0], acc[3][0]), y3 = fold32(acc[1][1], acc[3][1]);
    *reinterpret_cast<f32x2*>(lds + L_ACC + wave * D + 8 * c + 2 * r) = f32x2{fold16(y0, y2), fold16(y1, y3)} * fw;
  }
  const bool merger = s == NSPLIT - 1;
  u32x4 ga[NPOLL][NSPLIT - 1];
  u32x2 gm[NPOLL];
  auto poll = [&](int set) {
    asm volatile("" ::: "memory");
    gm[set] = __builtin_amdgcn_raw_buffer_load_b64(rw, (lane & 15) * 8, WS_ML + h * 128, AUX_SC1);
#pragma unroll
    for (int j = 0; j < NSPLIT - 1; ++j) ga[set][j] = __builtin_amdgcn_raw_buffer_load_b128(rw, voff + (j & 3) * 1024, WS_ACC + h * (NSPLIT * 1024) + (j >> 2) * 4096, AUX_SC1);
    asm volatile("" ::: "memory");
    __builtin_amdgcn_sched_barrier(0);
  };
  if (merger && wave == 0) poll(0);
  wg_barrier();
  if (wave == 0) {
    f32x2 o = *reinterpret_cast<const f32x2*>(lds + L_ACC + 2 * lane);
#pragma unroll
    for (int w = 1; w < WAVES; ++w) o += *reinterpret_cast<const f32x2*>(lds + L_ACC + w * D + 2 * lane);
    const f32x4 l4 = *reinterpret_cast<const f32x4*>(lds + L_SUM);
    const float L = (l4[0] + l4[1]) + (l4[2] + l4[3]);
    if (!merger) {
      __builtin_amdgcn_raw_buffer_store_b128(u32x4{__float_as_uint(o[0]), tag, __float_as_uint(o[1]), tag}, rw, voff, WS_ACC + blockIdx.x * 1024, AUX_SC1);
      __builtin_amdgcn_raw_buffer_store_b64(u32x2{__float_as_uint(L), tag}, rw, lane == 0 ? 0u : WS_END, WS_ML + h * 128 + 64 + s * 8, AUX_SC1);
    } else {
      const float own = (lane & 8) ? L : mwg;
      auto finish = [&](int set) {
        float ml = __uint_as_float(gm[set][0]);
        ml = (lane & 7) == NSPLIT - 1 ? own : ml;
        float M = ml;
        M = fmaxf(M, dpp<XOR1>(M)); M = fmaxf(M, dpp<XOR2>(M)); M = fmaxf(M, dpp<XOR7>(M));
        const float wt = __builtin_amdgcn_exp2f(ml - M);
        float LL = wt * dpp<XOR8>(ml);
        LL += dpp<XOR1>(LL); LL += dpp<XOR2>(LL); LL += dpp<XOR7>(LL);
        const float inv = 1.0f / lanef(LL, 0);
        f32x2 oo = o * lanef(wt, NSPLIT - 1);
#pragma unroll
        for (int j = 0; j < NSPLIT - 1; ++j) {
          const float wj = lanef(wt, j);
          oo = __builtin_elementwise_fma(f32x2{wj, wj}, f32x2{__uint_as_float(ga[set][j][0]), __uint_as_float(ga[set][j][2])}, oo);
        }
        oo *= inv;
        const __hip_bfloat16 b0 = __float2bfloat16(oo[0]), b1 = __float2bfloat16(oo[1]);
        reinterpret_cast<unsigned*>(out)[h * (D / 2) + lane] = (unsigned)__builtin_bit_cast(uint16_t, b0) | ((unsigned)__builtin_bit_cast(uint16_t, b1) << 16);
        if (lane == 0) *reinterpret_cast<unsigned*>(ws + WS_EPOCH + h * 256) = epoch + 1u;
      };
      auto complete = [&](int set) {
        bool ok = gm[set][1] == tag || (lane & 7) == NSPLIT - 1;
#pragma unroll
        for (int j = 0; j < NSPLIT - 1; ++j) ok &= ga[set][j][1] == tag && ga[set][j][3] == tag;
        return __all(ok);
      };
      for (int spin = 0; spin < (1 << 21); ++spin) {
        if constexpr (NPOLL == 2) {
          poll(1);
          if (complete(0)) { finish(0); break; }
          poll(0);
          if (complete(1)) { finish(1); break; }
        } else {
          if (complete(0)) { finish(0); break; }
          poll(0);
        }
      }
    }
  }
}

extern "C" void kernel_launch(void* const* d_in, const int* in_sizes, int n_in, void* d_out, int out_size, void* d_ws,
                              size_t ws_size, hipStream_t stream) {
  attn_decode<<<H * NSPLIT, NTHR, 0, stream>>>((const uint16_t*)d_in[0], (const uint16_t*)d_in[1], (const uint16_t*)d_in[2],
                                               (__hip_bfloat16*)d_out, (unsigned char*)d_ws);
}
```

```cpp
#define NX 3
#define THETA 20
#define TOUCH 0
#include <hip/hip_runtime.h>
#include <hip/hip_bf16.h>
#include <cstdint>
#include <type_traits>

#define CH 8
#define INF 6
#define SLOPE 2170
#define SOFF 120
#define NPOLL 1
#define X0 2
#define XS 10
#define XD 2
#define EARLYM 0
constexpr int H = 32, SK = 32768, D = 128, NSPLIT = 8, WAVES = 4, NTHR = (WAVES + 1) * 64;
constexpr uint32_t HEAD_BYTES = (uint32_t)SK * D * 2;
constexpr uint32_t KV_BYTES = (uint32_t)H * HEAD_BYTES;
constexpr uint32_t WG_BYTES = HEAD_BYTES / NSPLIT;
constexpr uint32_t GSTEP = 16 * 1024 * WAVES;
constexpr int NGRP = WG_BYTES / GSTEP;
static_assert(16 % CH == 0 && INF < 16, "");
constexpr uint32_t WS_EPOCH = 0, WS_ML = 16384, WS_REF = 32768, WS_ACC = 65536, WS_END = WS_ACC + H * NSPLIT * 1024;
constexpr int L_SC = 0, L_ACC = WAVES * NGRP * 64, L_MX = L_ACC + WAVES * D, L_SUM = L_MX + WAVES, L_ORG = L_SUM + WAVES,
              L_GRP = L_ORG + WAVES, L_REF = L_GRP + WAVES, L_END = L_REF + 1;

using bf16x2 = __attribute__((ext_vector_type(2))) __bf16;
using u32x2 = __attribute__((ext_vector_type(2))) unsigned;
using u32x4 = __attribute__((ext_vector_type(4))) unsigned;
using f32x4 = __attribute__((ext_vector_type(4))) float;
using f32x2 = __attribute__((ext_vector_type(2))) float;
using u64 = unsigned long long;
constexpr int AUX_NT = 2, AUX_SC1 = 16;
typedef __attribute__((address_space(3))) unsigned lu32;
#define RLX_WG __ATOMIC_RELAXED, __HIP_MEMORY_SCOPE_WORKGROUP

__device__ __forceinline__ float dot2(unsigned k, unsigned q, float acc) {
  return __builtin_amdgcn_fdot2_f32_bf16(__builtin_bit_cast(bf16x2, k), __builtin_bit_cast(bf16x2, q), acc, false);
}
template <int CTRL> __device__ __forceinline__ float dpp(float x) {
  return __builtin_bit_cast(float, __builtin_amdgcn_mov_dpp(__builtin_bit_cast(int, x), CTRL, 0xf, 0xf, true));
}
template <int CTRL> __device__ __forceinline__ int dppi(int x) { return __builtin_amdgcn_mov_dpp(x, CTRL, 0xf, 0xf, true); }
constexpr int XOR1 = 0xB1, XOR2 = 0x4E, XOR7 = 0x141, XOR8 = 0x128, ROWBCAST = 0x150;
__device__ __forceinline__ float fold32(float a, float b) {
  auto t = __builtin_amdgcn_permlane32_swap(__float_as_uint(a), __float_as_uint(b), false, false);
  return __uint_as_float(t[0]) + __uint_as_float(t[1]);
}
__device__ __forceinline__ float fold16(float a, float b) {
  auto t = __builtin_amdgcn_permlane16_swap(__float_as_uint(a), __float_as_uint(b), false, false);
  return __uint_as_float(t[0]) + __uint_as_float(t[1]);
}
__device__ __forceinline__ float wave_max(float x) {
  x = fmaxf(x, dpp<XOR1>(x)); x = fmaxf(x, dpp<XOR2>(x)); x = fmaxf(x, dpp<XOR7>(x)); x = fmaxf(x, dpp<XOR8>(x));
  auto s = __builtin_amdgcn_permlane16_swap(__float_as_uint(x), __float_as_uint(x), false, false);
  x = fmaxf(__uint_as_float(s[0]), __uint_as_float(s[1]));
  auto t = __builtin_amdgcn_permlane32_swap(__float_as_uint(x), __float_as_uint(x), false, false);
  return fmaxf(__uint_as_float(t[0]), __uint_as_float(t[1]));
}
__device__ __forceinline__ float wave_sum(float x) {
  x += dpp<XOR1>(x); x += dpp<XOR2>(x); x += dpp<XOR7>(x); x += dpp<XOR8>(x);
  x = fold16(x, x);
  return fold32(x, x);
}
__device__ __forceinline__ unsigned uni(unsigned x) { return __builtin_amdgcn_readfirstlane(x); }
__device__ __forceinline__ float lanef(float x, int l) { return __uint_as_float(__builtin_amdgcn_readlane(__float_as_uint(x), l)); }
template <int I, int N, class F> __device__ __forceinline__ void sfor(F&& f) {
  if constexpr (I < N) { f(std::integral_constant<int, I>{}); sfor<I + 1, N>(f); }
}
__device__ __forceinline__ void wg_barrier() {
  asm volatile("s_waitcnt lgkmcnt(0)\n\ts_barrier" ::: "memory");
}

__global__ void __launch_bounds__(NTHR, 1) attn_decode(const uint16_t* q, const uint16_t* k, const uint16_t* v,
                                                       __hip_bfloat16* out, unsigned char* ws) {
  __shared__ __attribute__((aligned(16))) float lds[L_END];
  const int h = blockIdx.x / NSPLIT, s = blockIdx.x % NSPLIT;
  const int tid = threadIdx.x, lane = tid & 63, wave = uni(tid >> 6), r = lane >> 4, c = lane & 15;
  constexpr float C2 = 0.088388347648318440f * 1.4426950408889634f;
  const uint32_t voff = lane * 16;
  const uint32_t base = blockIdx.x * WG_BYTES + wave * (CH * 1024);
  const auto rk = __builtin_amdgcn_make_buffer_rsrc((void*)k, 0, KV_BYTES, 0x00020000);
  const auto rv = __builtin_amdgcn_make_buffer_rsrc((void*)v, 0, KV_BYTES, 0x00020000);
  const auto rw = __builtin_amdgcn_make_buffer_rsrc((void*)ws, 0, WS_END, 0x00020000);
  lu32* const lw = (lu32*)lds;
  constexpr unsigned PENDING = 0x7fc0dead;
  if (wave == WAVES) {
    unsigned gep_v = __builtin_amdgcn_raw_buffer_load_b32(rw, 0, WS_EPOCH, AUX_SC1);
    const unsigned nominal = (unsigned)__builtin_amdgcn_s_memrealtime() + SOFF;
    if (lane < 4 * WAVES + 1)
      __hip_atomic_store(lw + L_MX + lane, lane < WAVES ? PENDING : (lane >= 2 * WAVES && lane < 3 * WAVES) || lane == 4 * WAVES ? nominal : 0u, RLX_WG);
    wg_barrier();
    __builtin_amdgcn_sched_barrier(0);
    asm volatile("" : "+v"(gep_v));
    const unsigned rtag = (uni(gep_v) ^ 0x2545f491u) * 0x9E3779B1u + 0x1b873593u;
    const int pj = blockIdx.x >> 4;
    const uint32_t pub_off = ((int)(blockIdx.x & 15) == (pj & 7) && lane == 0) ? pj * 8 : WS_END;
    int step = 0;
    for (int it = 0; it < 8192 && step < 2 * NX; ++it) {
      __builtin_amdgcn_s_sleep(12);
      const unsigned og = __hip_atomic_load(lw + L_ORG + (lane & 7), RLX_WG);
      int gmin = (int)og; gmin = min(gmin, dppi<XOR1>(gmin)); gmin = min(gmin, dppi<XOR2>(gmin));
      if (__builtin_amdgcn_readlane(gmin, 4) < X0 + (step >> 1) * XS + (step & 1) * XD) continue;
      asm volatile("" ::: "memory");
      if ((step & 1) == 0) {
        int d = (int)(og - nominal); d += dppi<XOR1>(d); d += dppi<XOR2>(d);
        const unsigned mine = nominal + (unsigned)(__builtin_amdgcn_readlane(d, 0) >> 2);
        __builtin_amdgcn_raw_buffer_store_b64(u32x2{mine, rtag}, rw, pub_off, WS_REF + (step >> 1) * 256, AUX_SC1);
      } else {
        const u32x2 ref = __builtin_amdgcn_raw_buffer_load_b64(rw, c * 8, WS_REF + (step >> 1) * 256, AUX_SC1);
        const bool ok = ref[1] == rtag;
        int e = ok ? (int)(ref[0] - nominal) : 0;
        e += dppi<XOR1>(e); e += dppi<XOR2>(e); e += dppi<XOR7>(e); e += dppi<XOR8>(e);
        if (__all(ok)) __hip_atomic_store(lw + L_REF, nominal + (unsigned)(e >> 4), RLX_WG);
      }
      ++step;
    }
    return;
  }
  u32x4 B[16];
  auto ld = [&](decltype(rk) rs, uint32_t a, auto I) {
    constexpr uint32_t i = decltype(I)::value, off = (i / CH) * (WAVES * CH * 1024) + (i % CH) * 1024;
    B[i] = __builtin_amdgcn_raw_buffer_load_b128(rs, voff + (off & 4095), a + (off & ~4095u), AUX_NT);
    __builtin_amdgcn_sched_barrier(0);
  };
  int lag;
  auto steer = [&](int G) {
    const unsigned own = (unsigned)__builtin_amdgcn_s_memrealtime() - (unsigned)(G * SLOPE) / 10u;
    lag = (int)(own - uni(__hip_atomic_load(lw + L_REF, RLX_WG))) - THETA;
    if (G > 0) __hip_atomic_store(lw + L_ORG + wave + (lane & 1) * WAVES, (lane & 1) ? (unsigned)G : own, RLX_WG);
  };
  auto arrived = [&](const u32x4& x, auto THROTTLE) {
    asm volatile("" :: "v"(x) : "memory");
    if constexpr (decltype(THROTTLE)::value)
      asm volatile("s_cmp_gt_i32 %0, 0\n\ts_cbranch_scc1 1f\n\ts_waitcnt vmcnt(%1)\n1:" :: "s"(lag), "i"(INF - 2) : "scc", "memory");
    __builtin_amdgcn_sched_barrier(0);
  };
  const u32x4 qv = *reinterpret_cast<const u32x4*>(q + h * D + 8 * c);
  __builtin_amdgcn_sched_barrier(0);
  sfor<0, INF>([&](auto I) { ld(rk, base, I); });
  const unsigned epoch_v = __builtin_amdgcn_raw_buffer_load_b32(rw, 0, WS_EPOCH + h * 256, AUX_SC1);
  lu32* const mx_slots = lw + L_MX;
  asm volatile("s_barrier" ::: "memory");
  float* const sl = lds + L_SC + wave * (NGRP * 64) + lane;
  float mx = -INFINITY;
  auto kgroup = [&](uint32_t cur, decltype(rk) rn, uint32_t next, int g) {
    float x[16];
    steer(g);
    sfor<0, 16>([&](auto I) {
      constexpr int i = I.value;
      arrived(B[i], std::true_type{});
      if constexpr (i + INF < 16) ld(rk, cur, std::integral_constant<int, (i + INF) % 16>{});
      else ld(rn, next, std::integral_constant<int, (i + INF) % 16>{});
      float d = 0.f;
#pragma unroll
      for (int j = 0; j < 4; ++j) d = dot2(B[i][j], qv[j], d);
      asm volatile("" : "+v"(d) :: "memory");
      x[i] = d;
      __builtin_amdgcn_sched_barrier(0);
    });
#define TR_STEP(HALF, CTRL, BIT) _Pragma("unroll") for (int j = 0; j < (HALF); ++j) { \
      const float lo_ = x[j] + dpp<CTRL>(x[j]), hi_ = x[j + (HALF)] + dpp<CTRL>(x[j + (HALF)]); x[j] = (c & (BIT)) ? hi_ : lo_; }
    TR_STEP(8, XOR8, 8) TR_STEP(4, XOR7, 4) TR_STEP(2, XOR2, 2) TR_STEP(1, XOR1, 1)
#undef TR_STEP
    mx = fmaxf(mx, x[0]);
    sl[64 * g] = x[0];
    __builtin_amdgcn_sched_barrier(0);
  };
#pragma unroll 1
  for (int g = 0; g < NGRP - 1; ++g) kgroup(base + g * GSTEP, rk, base + (g + 1) * GSTEP, g);
  kgroup(base + (NGRP - 1) * GSTEP, rv, base, NGRP - 1);
  const float m = wave_max(mx) * C2;
  if (lane == 0) __hip_atomic_store(mx_slots + wave, __float_as_uint(m), RLX_WG);
  const unsigned epoch = uni(epoch_v);
  const unsigned tag = (epoch ^ 0x5bd1e995u) * 0x9E3779B1u + 0x7f4a7c15u;
  float mwg, fw;
  float l = 0.f;
  f32x2 acc[4];
#pragma unroll
  for (int i = 0; i < 4; ++i) acc[i] = f32x2{0.f, 0.f};
  auto vgroup = [&](uint32_t cur, int g, auto LAST) {
    constexpr bool last = decltype(LAST)::value;
    steer(NGRP + g);
    const float p = __builtin_amdgcn_exp2f(fmaf(sl[64 * g], C2, -m));
    l += p;
    sfor<0, 16>([&](auto I) {
      constexpr int i = I.value;
      arrived(B[i], std::integral_constant<bool, !last || i + INF < 16>{});
      if constexpr (i + INF < 16) ld(rv, cur, std::integral_constant<int, (i + INF) % 16>{});
      else if constexpr (!last) ld(rv, cur + GSTEP, std::integral_constant<int, (i + INF) % 16>{});
      if constexpr (last && i == 15 - INF) {
        unsigned u[WAVES];
        for (int spin = 0; spin < (1 << 20); ++spin) {
#pragma unroll
          for (int w = 0; w < WAVES; ++w) u[w] = __hip_atomic_load(mx_slots + w, RLX_WG);
          if (uni((u[0] == PENDING) | (u[1] == PENDING) | (u[2] == PENDING) | (u[3] == PENDING)) == 0) break;
        }
        mwg = fmaxf(fmaxf(__uint_as_float(u[0]), __uint_as_float(u[1])), fmaxf(__uint_as_float(u[2]), __uint_as_float(u[3])));
        fw = __builtin_amdgcn_exp2f(m - mwg);
        l = wave_sum(l) * fw;
        if (lane == 0) lds[L_SUM + wave] = l;
        if (EARLYM) __builtin_amdgcn_raw_buffer_store_b64(u32x2{__float_as_uint(mwg), tag}, rw, tid == 0 ? 0u : WS_END, WS_ML + h * 128 + s * 8, AUX_SC1);
        __builtin_amdgcn_sched_barrier(0);
      }
      const float pb = dpp<ROWBCAST + i>(p);
#pragma unroll
      for (int j = 0; j < 4; ++j) {
        const f32x2 vv = {__builtin_bit_cast(float, B[i][j] << 16), __builtin_bit_cast(float, B[i][j] & 0xffff0000u)};
        acc[j] = __builtin_elementwise_fma(f32x2{pb, pb}, vv, acc[j]);
      }
#pragma unroll
      for (int j = 0; j < 4; ++j) asm volatile("" : "+v"(acc[j]) :: "memory");
      __builtin_amdgcn_sched_barrier(0);
    });
  };
#pragma unroll 1
  for (int g = 0; g < NGRP - 1; ++g) vgroup(base + g * GSTEP, g, std::false_type{});
  vgroup(base + (NGRP - 1) * GSTEP, NGRP - 1, std::true_type{});
  {
    const float y0 = fold32(acc[0][0], acc[2][0]), y1 = fold32(acc[0][1], acc[2][1]);
    const float y2 = fold32(acc[1][0], acc[3][0]), y3 = fold32(acc[1][1], acc[3][1]);
    *reinterpret_cast<f32x2*>(lds + L_ACC + wave * D + 8 * c + 2 * r) = f32x2{fold16(y0, y2), fold16(y1, y3)} * fw;
  }
  const bool merger = s == NSPLIT - 1;
  u32x4 ga[NPOLL][NSPLIT - 1];
  u32x2 gm[NPOLL];
  auto poll = [&](int set) {
    asm volatile("" ::: "memory");
    gm[set] = __builtin_amdgcn_raw_buffer_load_b64(rw, (lane & 15) * 8, WS_ML + h * 128, AUX_SC1);
#pragma unroll
    for (int j = 0; j < NSPLIT - 1; ++j) ga[set][j] = __builtin_amdgcn_raw_buffer_load_b128(rw, voff + (j & 3) * 1024, WS_ACC + h * (NSPLIT * 1024) + (j >> 2) * 4096, AUX_SC1);
    asm volatile("" ::: "memory");
    __builtin_amdgcn_sched_barrier(0);
  };
  if (merger && wave == 0) poll(0);
  wg_barrier();
  if (wave == 0) {
    f32x2 o = *reinterpret_cast<const f32x2*>(lds + L_ACC + 2 * lane);
#pragma unroll
    for (int w = 1; w < WAVES; ++w) o += *reinterpret_cast<const f32x2*>(lds + L_ACC + w * D + 2 * lane);
    const f32x4 l4 = *reinterpret_cast<const f32x4*>(lds + L_SUM);
    const float L = (l4[0] + l4[1]) + (l4[2] + l4[3]);
    if (!merger) {
      __builtin_amdgcn_raw_buffer_store_b128(u32x4{__float_as_uint(o[0]), tag, __float_as_uint(o[1]), tag}, rw, voff, WS_ACC + blockIdx.x * 1024, AUX_SC1);
      if (EARLYM) __builtin_amdgcn_raw_buffer_store_b64(u32x2{__float_as_uint(L), tag}, rw, lane == 0 ? 0u : WS_END, WS_ML + h * 128 + 64 + s * 8, AUX_SC1);
      else __builtin_amdgcn_raw_buffer_store_b64(u32x2{__float_as_uint(lane ? L : mwg), tag}, rw, lane < 2 ? lane * 64u : WS_END, WS_ML + h * 128 + s * 8, AUX_SC1);
    } else {
      const float own = (lane & 8) ? L : mwg;
      auto finish = [&](int set) {
        float ml = __uint_as_float(gm[set][0]);
        ml = (lane & 7) == NSPLIT - 1 ? own : ml;
        float M = ml;
        M = fmaxf(M, dpp<XOR1>(M)); M = fmaxf(M, dpp<XOR2>(M)); M = fmaxf(M, dpp<XOR7>(M));
        const float wt = __builtin_amdgcn_exp2f(ml - M);
        float LL = wt * dpp<XOR8>(ml);
        LL += dpp<XOR1>(LL); LL += dpp<XOR2>(LL); LL += dpp<XOR7>(LL);
        const float inv = 1.0f / lanef(LL, 0);
        f32x2 oo = o * lanef(wt, NSPLIT - 1);
#pragma unroll
        for (int j = 0; j < NSPLIT - 1; ++j) {
          const float wj = lanef(wt, j);
          oo = __builtin_elementwise_fma(f32x2{wj, wj}, f32x2{__uint_as_float(ga[set][j][0]), __uint_as_float(ga[set][j][2])}, oo);
        }
        oo *= inv;
        const __hip_bfloat16 b0 = __float2bfloat16(oo[0]), b1 = __float2bfloat16(oo[1]);
        reinterpret_cast<unsigned*>(out)[h * (D / 2) + lane] = (unsigned)__builtin_bit_cast(uint16_t, b0) | ((unsigned)__builtin_bit_cast(uint16_t, b1) << 16);
        if (lane == 0) *reinterpret_cast<unsigned*>(ws + WS_EPOCH + h * 256) = epoch + 1u;
      };
      auto complete = [&](int set) {
        bool ok = gm[set][1] == tag || (lane & 7) == NSPLIT - 1;
#pragma unroll
        for (int j = 0; j < NSPLIT - 1; ++j) ok &= ga[set][j][1] == tag && ga[set][j][3] == tag;
        return __all(ok);
      };
      for (int spin = 0; spin < (1 << 21); ++spin) {
        if constexpr (NPOLL == 2) {
          poll(1);
          if (complete(0)) { finish(0); break; }
          poll(0);
          if (complete(1)) { finish(1); break; }
        } else {
          if (complete(0)) { finish(0); break; }
          poll(0);
        }
      }
    }
  }
}

extern "C" void kernel_launch(void* const* d_in, const int* in_sizes, int n_in, void* d_out, int out_size, void* d_ws,
                              size_t ws_size, hipStream_t stream) {
  attn_decode<<<H * NSPLIT, NTHR, 0, stream>>>((const uint16_t*)d_in[0], (const uint16_t*)d_in[1], (const uint16_t*)d_in[2],
                                               (__hip_bfloat16*)d_out, (unsigned char*)d_ws);
}
```

```cpp
#include <hip/hip_runtime.h>
#include <hip/hip_bf16.h>
#include <cstdint>
#include <type_traits>

#define MASKS 0x20, 0x608, 0x40, 0x506, 0x313, 0x290, 0xc1, 0x1020, 0x48, 0x23, 0x201, 0x9, 0xa00, 0x0, 0x29, 0x0, 0x400, 0x10
#define SLOPE 2130
#define THETA 40
constexpr int INF = 6;
constexpr int SOFF = 120;
constexpr int NX = 3, X0 = 2, XS = 10, XD = 2;

constexpr int H = 32, SK = 32768, D = 128, NSPLIT = 8, WAVES = 4, NTHR = (WAVES + 1) * 64, NGRP = 16;
constexpr uint32_t HEAD_BYTES = (uint32_t)SK * D * 2;
constexpr uint32_t KV_BYTES = (uint32_t)H * HEAD_BYTES;
static_assert(NSPLIT * WAVES * NGRP * 16 * 1024 == HEAD_BYTES, "splits x waves x groups x loads cover a head");
constexpr uint32_t AM[18] = {MASKS};
constexpr uint32_t spread(uint32_t x, int first, int n) {
  uint32_t a = 0;
  for (int j = 0; j < n; ++j) if (x >> j & 1) a ^= AM[first + j] << 10;
  return a;
}
constexpr bool map_is_bijective() {
  uint32_t v[13] = {};
  for (int i = 0; i < 13; ++i) v[i] = AM[i];
  for (int i = 0; i < 18; ++i) if (AM[i] >> 13) return false;
  for (int b = 0; b < 13; ++b) {
    int p = -1;
    for (int i = b; i < 13 && p < 0; ++i) if (v[i] >> b & 1) p = i;
    if (p < 0) return false;
    const uint32_t t = v[p]; v[p] = v[b]; v[b] = t;
    for (int i = 0; i < 13; ++i) if (i != b && (v[i] >> b & 1)) v[i] ^= v[b];
  }
  return true;
}
static_assert(map_is_bijective(), "");
constexpr uint32_t WS_EPOCH = 0, WS_ML = 16384, WS_REF = 32768, WS_ACC = 65536, WS_END = WS_ACC + H * NSPLIT * 1024;
constexpr int L_SC = 0, L_ACC = WAVES * NGRP * 64, L_MX = L_ACC + WAVES * D, L_SUM = L_MX + WAVES, L_ORG = L_SUM + WAVES,
              L_GRP = L_ORG + WAVES, L_REF = L_GRP + WAVES, L_END = L_REF + 1;

using bf16x2 = __attribute__((ext_vector_type(2))) __bf16;
using u32x2 = __attribute__((ext_vector_type(2))) unsigned;
using u32x4 = __attribute__((ext_vector_type(4))) unsigned;
using f32x4 = __attribute__((ext_vector_type(4))) float;
using f32x2 = __attribute__((ext_vector_type(2))) float;
typedef __attribute__((address_space(3))) unsigned lu32;
#define RLX_WG __ATOMIC_RELAXED, __HIP_MEMORY_SCOPE_WORKGROUP
constexpr int AUX_NT = 2, AUX_SC1 = 16;

__device__ __forceinline__ float dot2(unsigned k, unsigned q, float acc) {
  return __builtin_amdgcn_fdot2_f32_bf16(__builtin_bit_cast(bf16x2, k), __builtin_bit_cast(bf16x2, q), acc, false);
}
template <int CTRL> __device__ __forceinline__ int dppi(int x) { return __builtin_amdgcn_mov_dpp(x, CTRL, 0xf, 0xf, true); }
template <int CTRL> __device__ __forceinline__ float dpp(float x) { return __builtin_bit_cast(float, dppi<CTRL>(__builtin_bit_cast(int, x))); }
constexpr int XOR1 = 0xB1, XOR2 = 0x4E, XOR7 = 0x141, XOR8 = 0x128, ROWBCAST = 0x150;
__device__ __forceinline__ float fold32(float a, float b) {
  auto t = __builtin_amdgcn_permlane32_swap(__float_as_uint(a), __float_as_uint(b), false, false);
  return __uint_as_float(t[0]) + __uint_as_float(t[1]);
}
__device__ __forceinline__ float fold16(float a, float b) {
  auto t = __builtin_amdgcn_permlane16_swap(__float_as_uint(a), __float_as_uint(b), false, false);
  return __uint_as_float(t[0]) + __uint_as_float(t[1]);
}
__device__ __forceinline__ float wave_max(float x) {
  x = fmaxf(x, dpp<XOR1>(x)); x = fmaxf(x, dpp<XOR2>(x)); x = fmaxf(x, dpp<XOR7>(x)); x = fmaxf(x, dpp<XOR8>(x));
  auto s = __builtin_amdgcn_permlane16_swap(__float_as_uint(x), __float_as_uint(x), false, false);
  x = fmaxf(__uint_as_float(s[0]), __uint_as_float(s[1]));
  auto t = __builtin_amdgcn_permlane32_swap(__float_as_uint(x), __float_as_uint(x), false, false);
  return fmaxf(__uint_as_float(t[0]), __uint_as_float(t[1]));
}
__device__ __forceinline__ float wave_sum(float x) {
  x += dpp<XOR1>(x); x += dpp<XOR2>(x); x += dpp<XOR7>(x); x += dpp<XOR8>(x);
  x = fold16(x, x);
  return fold32(x, x);
}
template <int CTRL, int BIT> __device__ __forceinline__ float tr(float a, float b, int c) {
  const float lo = a + dpp<CTRL>(a), hi = b + dpp<CTRL>(b);
  return (c & BIT) ? hi : lo;
}
__device__ __forceinline__ unsigned uni(unsigned x) { return __builtin_amdgcn_readfirstlane(x); }
__device__ __forceinline__ float lanef(float x, int l) { return __uint_as_float(__builtin_amdgcn_readlane(__float_as_uint(x), l)); }
__device__ __forceinline__ unsigned now() { return (unsigned)__builtin_amdgcn_s_memrealtime(); }
template <int I, int N, class F> __device__ __forceinline__ void sfor(F&& f) {
  if constexpr (I < N) { f(std::integral_constant<int, I>{}); sfor<I + 1, N>(f); }
}
template <int N> using ic = std::integral_constant<int, N>;
__device__ __forceinline__ void wg_barrier() {
  asm volatile("s_waitcnt lgkmcnt(0)\n\ts_barrier" ::: "memory");
}
__device__ __forceinline__ unsigned mix(unsigned e) { return (e ^ 0x5bd1e995u) * 0x9E3779B1u + 0x7f4a7c15u; }
constexpr unsigned PENDING = 0x7fc0dead;

__global__ void __launch_bounds__(NTHR, 1) attn_decode(const uint16_t* q, const uint16_t* k, const uint16_t* v,
                                                       __hip_bfloat16* out, unsigned char* ws) {
  __shared__ __attribute__((aligned(16))) float lds[L_END];
  asm volatile("" :: "s"(q), "s"(k), "s"(v), "s"(out), "s"(ws));
  const int h = blockIdx.x / NSPLIT, s = blockIdx.x % NSPLIT;
  const int tid = threadIdx.x, lane = tid & 63, wave = uni(tid >> 6), r = lane >> 4, c = lane & 15;
  const uint32_t voff = lane * 16;
  const auto rw = __builtin_amdgcn_make_buffer_rsrc((void*)ws, 0, WS_END, 0x00020000);
  lu32* const lw = (lu32*)lds;

  if (wave == WAVES) {
    unsigned gep_v = __builtin_amdgcn_raw_buffer_load_b32(rw, 0, WS_EPOCH, AUX_SC1);
    const unsigned nominal = now() + SOFF;
    if (lane < 4 * WAVES + 1)
      __hip_atomic_store(lw + L_MX + lane, lane < WAVES ? PENDING : (lane >= 2 * WAVES && lane < 3 * WAVES) || lane == 4 * WAVES ? nominal : 0u, RLX_WG);
    wg_barrier();
    __builtin_amdgcn_sched_barrier(0);
    asm volatile("" : "+v"(gep_v));
    const unsigned rtag = mix(uni(gep_v)) ^ 0x68e31da4u;
    const int pj = blockIdx.x >> 4;
    const uint32_t pub_off = ((int)(blockIdx.x & 15) == (pj & 7) && lane == 0) ? pj * 8 : WS_END;
    int step = 0;
    for (int it = 0; it < 8192 && step < 2 * NX; ++it) {
      __builtin_amdgcn_s_sleep(12);
      const unsigned og = __hip_atomic_load(lw + L_ORG + (lane & 7), RLX_WG);
      int gmin = (int)og; gmin = min(gmin, dppi<XOR1>(gmin)); gmin = min(gmin, dppi<XOR2>(gmin));
      if (__builtin_amdgcn_readlane(gmin, 4) < X0 + (step >> 1) * XS + (step & 1) * XD) continue;
      asm volatile("" ::: "memory");
      if ((step & 1) == 0) {
        int d = (int)(og - nominal); d += dppi<XOR1>(d); d += dppi<XOR2>(d);
        const unsigned mine = nominal + (unsigned)(__builtin_amdgcn_readlane(d, 0) >> 2);
        __builtin_amdgcn_raw_buffer_store_b64(u32x2{mine, rtag}, rw, pub_off, WS_REF + (step >> 1) * 256, AUX_SC1);
      } else {
        const u32x2 ref = __builtin_amdgcn_raw_buffer_load_b64(rw, c * 8, WS_REF + (step >> 1) * 256, AUX_SC1);
        const bool ok = ref[1] == rtag;
        int e = ok ? (int)(ref[0] - nominal) : 0;
        e += dppi<XOR1>(e); e += dppi<XOR2>(e); e += dppi<XOR7>(e); e += dppi<XOR8>(e);
        if (__all(ok)) __hip_atomic_store(lw + L_REF, nominal + (unsigned)(e >> 4), RLX_WG);
      }
      ++step;
    }
    return;
  }

  constexpr float C2 = 0.088388347648318440f * 1.4426950408889634f;
  const uint32_t base = h * HEAD_BYTES ^ spread(wave, 8, 2) ^ spread(s, 10, 3) ^ spread(h, 13, 5);
  const auto rk = __builtin_amdgcn_make_buffer_rsrc((void*)k, 0, KV_BYTES, 0x00020000);
  const auto rv = __builtin_amdgcn_make_buffer_rsrc((void*)v, 0, KV_BYTES, 0x00020000);
  u32x4 B[16];
  auto ld = [&](decltype(rk) rs, uint32_t a, auto I) {
    B[I.value] = __builtin_amdgcn_raw_buffer_load_b128(rs, voff, a ^ spread(I.value, 0, 4), AUX_NT);
    __builtin_amdgcn_sched_barrier(0);
  };
  int lag;
  auto steer = [&](int G) {
    const unsigned own = now() - (unsigned)(G * SLOPE) / 10u;
    lag = (int)(own - uni(__hip_atomic_load(lw + L_REF, RLX_WG))) - THETA;
    if (G > 0) __hip_atomic_store(lw + L_ORG + wave + (lane & 1) * WAVES, (lane & 1) ? (unsigned)G : own, RLX_WG);
  };
  auto arrived = [&](const u32x4& x, auto THROTTLE) {
    asm volatile("" :: "v"(x) : "memory");
    if constexpr (THROTTLE.value)
      asm volatile("s_cmp_gt_i32 %0, 0\n\ts_cbranch_scc1 1f\n\ts_waitcnt vmcnt(%1)\n1:" :: "s"(lag), "i"(INF - 2) : "scc", "memory");
    __builtin_amdgcn_sched_barrier(0);
  };
  const u32x4 qv = *reinterpret_cast<const u32x4*>(q + h * D + 8 * c);
  __builtin_amdgcn_sched_barrier(0);
  sfor<0, INF>([&](auto I) { ld(rk, base, I); });
  const unsigned epoch_v = __builtin_amdgcn_raw_buffer_load_b32(rw, 0, WS_EPOCH + h * 256, AUX_SC1);
  asm volatile("s_barrier" ::: "memory");
  float* const sl = lds + L_SC + wave * (NGRP * 64) + lane;
  float mx = -INFINITY;
  auto kgroup = [&](uint32_t cur, decltype(rk) rn, uint32_t next, int g) {
    float x[4];
    steer(g);
    sfor<0, 16>([&](auto I) {
      constexpr int i = I.value;
      arrived(B[i], std::true_type{});
      if constexpr (i + INF < 16) ld(rk, cur, ic<(i + INF) % 16>{});
      else ld(rn, next, ic<(i + INF) % 16>{});
      float d = 0.f;
#pragma unroll
      for (int j = 0; j < 4; ++j) d = dot2(B[i][j], qv[j], d);
      if constexpr ((i & 1) == 1) d = tr<XOR8, 8>(x[0], d, c);
      if constexpr ((i & 3) == 3) d = tr<XOR7, 4>(x[1], d, c);
      if constexpr ((i & 7) == 7) d = tr<XOR2, 2>(x[2], d, c);
      if constexpr (i == 15) d = tr<XOR1, 1>(x[3], d, c);
      asm volatile("" : "+v"(d) :: "memory");
      x[(i & 1) == 0 ? 0 : (i & 3) == 1 ? 1 : (i & 7) == 3 ? 2 : i == 7 ? 3 : 0] = d;
      __builtin_amdgcn_sched_barrier(0);
    });
    mx = fmaxf(mx, x[0]);
    sl[64 * g] = x[0];
    __builtin_amdgcn_sched_barrier(0);
  };
#pragma unroll 1
  for (int g = 0; g < NGRP - 1; ++g) kgroup(base ^ spread(g, 4, 4), rk, base ^ spread(g + 1, 4, 4), g);
  kgroup(base ^ spread(NGRP - 1, 4, 4), rv, base, NGRP - 1);
  const float m = wave_max(mx) * C2;
  if (lane == 0) __hip_atomic_store(lw + L_MX + wave, __float_as_uint(m), RLX_WG);
  const unsigned epoch = uni(epoch_v), tag = mix(epoch);
  float mwg, fw;
  float l = 0.f;
  f32x2 acc[4];
#pragma unroll
  for (int i = 0; i < 4; ++i) acc[i] = f32x2{0.f, 0.f};
  auto vgroup = [&](uint32_t cur, uint32_t next, int g, auto LAST) {
    constexpr bool last = LAST.value;
    steer(NGRP + g);
    const float p = __builtin_amdgcn_exp2f(fmaf(sl[64 * g], C2, -m));
    l += p;
    sfor<0, 16>([&](auto I) {
      constexpr int i = I.value;
      arrived(B[i], std::integral_constant<bool, !last || i + INF < 16>{});
      if constexpr (i + INF < 16) ld(rv, cur, ic<(i + INF) % 16>{});
      else if constexpr (!last) ld(rv, next, ic<(i + INF) % 16>{});
      if constexpr (last && i == 15 - INF) {
        unsigned u[WAVES];
        for (int spin = 0; spin < (1 << 20); ++spin) {
#pragma unroll
          for (int w = 0; w < WAVES; ++w) u[w] = __hip_atomic_load(lw + L_MX + w, RLX_WG);
          if (uni((u[0] == PENDING) | (u[1] == PENDING) | (u[2] == PENDING) | (u[3] == PENDING)) == 0) break;
        }
        mwg = fmaxf(fmaxf(__uint_as_float(u[0]), __uint_as_float(u[1])), fmaxf(__uint_as_float(u[2]), __uint_as_float(u[3])));
        fw = __builtin_amdgcn_exp2f(m - mwg);
        l = wave_sum(l) * fw;
        if (lane == 0) lds[L_SUM + wave] = l;
        __builtin_amdgcn_sched_barrier(0);
      }
      const float pb = dpp<ROWBCAST + ((i & 1) << 3 | (i & 2) << 1 | (i & 4) >> 1 | (i & 8) >> 3)>(p);
#pragma unroll
      for (int j = 0; j < 4; ++j) {
        const f32x2 vv = {__builtin_bit_cast(float, B[i][j] << 16), __builtin_bit_cast(float, B[i][j] & 0xffff0000u)};
        acc[j] = __builtin_elementwise_fma(f32x2{pb, pb}, vv, acc[j]);
      }
#pragma unroll
      for (int j = 0; j < 4; ++j) asm volatile("" : "+v"(acc[j]) :: "memory");
      __builtin_amdgcn_sched_barrier(0);
    });
  };
#pragma unroll 1
  for (int g = 0; g < NGRP - 1; ++g) vgroup(base ^ spread(g, 4, 4), base ^ spread(g + 1, 4, 4), g, std::false_type{});
  vgroup(base ^ spread(NGRP - 1, 4, 4), 0, NGRP - 1, std::true_type{});
  {
    const float y0 = fold32(acc[0][0], acc[2][0]), y1 = fold32(acc[0][1], acc[2][1]);
    const float y2 = fold32(acc[1][0], acc[3][0]), y3 = fold32(acc[1][1], acc[3][1]);
    *reinterpret_cast<f32x2*>(lds + L_ACC + wave * D + 8 * c + 2 * r) = f32x2{fold16(y0, y2), fold16(y1, y3)} * fw;
  }
  const bool merger = s == NSPLIT - 1;
  u32x4 ga[NSPLIT - 1];
  u32x2 gm;
  auto poll = [&]() {
    asm volatile("" ::: "memory");
    gm = __builtin_amdgcn_raw_buffer_load_b64(rw, c * 8, WS_ML + h * 128, AUX_SC1);
#pragma unroll
    for (int j = 0; j < NSPLIT - 1; ++j)
      ga[j] = __builtin_amdgcn_raw_buffer_load_b128(rw, voff + (j & 3) * 1024, WS_ACC + h * (NSPLIT * 1024) + (j >> 2) * 4096, AUX_SC1);
    asm volatile("" ::: "memory");
    __builtin_amdgcn_sched_barrier(0);
  };
  if (merger && wave == 0) poll();
  wg_barrier();
  if (wave != 0) return;
  f32x2 o = *reinterpret_cast<const f32x2*>(lds + L_ACC + 2 * lane);
#pragma unroll
  for (int w = 1; w < WAVES; ++w) o += *reinterpret_cast<const f32x2*>(lds + L_ACC + w * D + 2 * lane);
  const f32x4 l4 = *reinterpret_cast<const f32x4*>(lds + L_SUM);
  const float L = (l4[0] + l4[1]) + (l4[2] + l4[3]);
  if (!merger) {
    __builtin_amdgcn_raw_buffer_store_b128(u32x4{__float_as_uint(o[0]), tag, __float_as_uint(o[1]), tag}, rw, voff, WS_ACC + blockIdx.x * 1024, AUX_SC1);
    __builtin_amdgcn_raw_buffer_store_b64(u32x2{__float_as_uint(lane ? L : mwg), tag}, rw, lane < 2 ? lane * 64u : WS_END, WS_ML + h * 128 + s * 8, AUX_SC1);
    return;
  }
  for (int spin = 0; spin < (1 << 21); ++spin) {
    unsigned lo = (lane & 7) == NSPLIT - 1 ? tag : gm[1], hi = lo;
#pragma unroll
    for (int j = 0; j < NSPLIT - 1; ++j) { lo = min(lo, min(ga[j][1], ga[j][3])); hi = max(hi, max(ga[j][1], ga[j][3])); }
    if (__all(lo == tag && hi == tag)) break;
    poll();
  }
  __builtin_amdgcn_raw_buffer_store_b32(epoch + 1u, rw, lane == 0 ? 0u : WS_END, WS_EPOCH + h * 256, AUX_SC1);
  __builtin_amdgcn_sched_barrier(0);
  const float ml = (lane & 7) == NSPLIT - 1 ? ((lane & 8) ? L : mwg) : __uint_as_float(gm[0]);
  float M = ml;
  M = fmaxf(M, dpp<XOR1>(M)); M = fmaxf(M, dpp<XOR2>(M)); M = fmaxf(M, dpp<XOR7>(M));
  const float wt = __builtin_amdgcn_exp2f(ml - M);
  float LL = wt * dpp<XOR8>(ml);
  LL += dpp<XOR1>(LL); LL += dpp<XOR2>(LL); LL += dpp<XOR7>(LL);
  const float inv = 1.0f / lanef(LL, 0);
  f32x2 oo = o * lanef(wt, NSPLIT - 1);
#pragma unroll
  for (int j = 0; j < NSPLIT - 1; ++j) {
    const float wj = lanef(wt, j);
    oo = __builtin_elementwise_fma(f32x2{wj, wj}, f32x2{__uint_as_float(ga[j][0]), __uint_as_float(ga[j][2])}, oo);
  }
  oo *= inv;
  const __hip_bfloat16 b0 = __float2bfloat16(oo[0]), b1 = __float2bfloat16(oo[1]);
  __builtin_amdgcn_raw_buffer_store_b32((unsigned)__builtin_bit_cast(uint16_t, b0) | ((unsigned)__builtin_bit_cast(uint16_t, b1) << 16),
                                        __builtin_amdgcn_make_buffer_rsrc((void*)out, 0, H * D * 2, 0x00020000), lane * 4, h * D * 2, AUX_SC1);
}

extern "C" void kernel_launch(void* const* d_in, const int* in_sizes, int n_in, void* d_out, int out_size, void* d_ws,
                              size_t ws_size, hipStream_t stream) {
  attn_decode<<<H * NSPLIT, NTHR, 0, stream>>>((const uint16_t*)d_in[0], (const uint16_t*)d_in[1], (const uint16_t*)d_in[2],
                                               (__hip_bfloat16*)d_out, (unsigned char*)d_ws);
}
```

```cpp
#include <hip/hip_runtime.h>
#include <hip/hip_bf16.h>
#include <cstdint>
#include <type_traits>

#define MASKS 0x20, 0x608, 0x40, 0x546, 0x2d0, 0x353, 0x81, 0x1020, 0x23, 0x48, 0x840, 0x9, 0x1a02, 0x0, 0x0, 0x0, 0x400, 0x10
#define SLOPE 2120
#define THETA 30
constexpr int INF = 6;
constexpr int SOFF = 120;
constexpr int NX = 3, X0 = 2, XS = 10, XD = 2;
constexpr int GEXIT = 29, DMAX = 160;

constexpr int H = 32, SK = 32768, D = 128, NSPLIT = 8, WAVES = 4, NTHR = (WAVES + 1) * 64, NGRP = 16;
constexpr uint32_t HEAD_BYTES = (uint32_t)SK * D * 2;
constexpr uint32_t KV_BYTES = (uint32_t)H * HEAD_BYTES;
static_assert(NSPLIT * WAVES * NGRP * 16 * 1024 == HEAD_BYTES, "splits x waves x groups x loads cover a head");
constexpr uint32_t AM[18] = {MASKS};
constexpr uint32_t spread(uint32_t x, int first, int n) {
  uint32_t a = 0;
  for (int j = 0; j < n; ++j) if (x >> j & 1) a ^= AM[first + j] << 10;
  return a;
}
constexpr bool map_is_bijective() {
  uint32_t v[13] = {};
  for (int i = 0; i < 13; ++i) v[i] = AM[i];
  for (int i = 0; i < 18; ++i) if (AM[i] >> 13) return false;
  for (int b = 0; b < 13; ++b) {
    int p = -1;
    for (int i = b; i < 13 && p < 0; ++i) if (v[i] >> b & 1) p = i;
    if (p < 0) return false;
    const uint32_t t = v[p]; v[p] = v[b]; v[b] = t;
    for (int i = 0; i < 13; ++i) if (i != b && (v[i] >> b & 1)) v[i] ^= v[b];
  }
  return true;
}
static_assert(map_is_bijective(), "");
constexpr uint32_t WS_EPOCH = 0, WS_ML = 16384, WS_REF = 32768, WS_ACC = 65536, WS_END = WS_ACC + H * NSPLIT * 1024;
constexpr int L_SC = 0, L_ACC = WAVES * NGRP * 64, L_MX = L_ACC + WAVES * D, L_SUM = L_MX + WAVES, L_ORG = L_SUM + WAVES,
              L_GRP = L_ORG + WAVES, L_REF = L_GRP + WAVES, L_END = L_REF + 1;

using bf16x2 = __attribute__((ext_vector_type(2))) __bf16;
using u32x2 = __attribute__((ext_vector_type(2))) unsigned;
using u32x4 = __attribute__((ext_vector_type(4))) unsigned;
using f32x4 = __attribute__((ext_vector_type(4))) float;
using f32x2 = __attribute__((ext_vector_type(2))) float;
using u64 = unsigned long long;
using u64x4 = __attribute__((ext_vector_type(4))) u64;
typedef __attribute__((address_space(1))) u32x4 gu32x4;
typedef __attribute__((address_space(3))) unsigned lu32;
#define RLX_WG __ATOMIC_RELAXED, __HIP_MEMORY_SCOPE_WORKGROUP
constexpr int AUX_NT = 2, AUX_SC1 = 16;

__device__ __forceinline__ float dot2(unsigned k, unsigned q, float acc) {
  return __builtin_amdgcn_fdot2_f32_bf16(__builtin_bit_cast(bf16x2, k), __builtin_bit_cast(bf16x2, q), acc, false);
}
template <int CTRL> __device__ __forceinline__ int dppi(int x) { return __builtin_amdgcn_mov_dpp(x, CTRL, 0xf, 0xf, true); }
template <int CTRL> __device__ __forceinline__ float dpp(float x) { return __builtin_bit_cast(float, dppi<CTRL>(__builtin_bit_cast(int, x))); }
constexpr int XOR1 = 0xB1, XOR2 = 0x4E, XOR7 = 0x141, XOR8 = 0x128, ROWBCAST = 0x150;
__device__ __forceinline__ float fold32(float a, float b) {
  auto t = __builtin_amdgcn_permlane32_swap(__float_as_uint(a), __float_as_uint(b), false, false);
  return __uint_as_float(t[0]) + __uint_as_float(t[1]);
}
__device__ __forceinline__ float fold16(float a, float b) {
  auto t = __builtin_amdgcn_permlane16_swap(__float_as_uint(a), __float_as_uint(b), false, false);
  return __uint_as_float(t[0]) + __uint_as_float(t[1]);
}
__device__ __forceinline__ float wave_max(float x) {
  x = fmaxf(x, dpp<XOR1>(x)); x = fmaxf(x, dpp<XOR2>(x)); x = fmaxf(x, dpp<XOR7>(x)); x = fmaxf(x, dpp<XOR8>(x));
  auto s = __builtin_amdgcn_permlane16_swap(__float_as_uint(x), __float_as_uint(x), false, false);
  x = fmaxf(__uint_as_float(s[0]), __uint_as_float(s[1]));
  auto t = __builtin_amdgcn_permlane32_swap(__float_as_uint(x), __float_as_uint(x), false, false);
  return fmaxf(__uint_as_float(t[0]), __uint_as_float(t[1]));
}
__device__ __forceinline__ float wave_sum(float x) {
  x += dpp<XOR1>(x); x += dpp<XOR2>(x); x += dpp<XOR7>(x); x += dpp<XOR8>(x);
  x = fold16(x, x);
  return fold32(x, x);
}
template <int CTRL, int BIT> __device__ __forceinline__ float tr(float a, float b, int c) {
  const float lo = a + dpp<CTRL>(a), hi = b + dpp<CTRL>(b);
  return (c & BIT) ? hi : lo;
}
__device__ __forceinline__ unsigned uni(unsigned x) { return __builtin_amdgcn_readfirstlane(x); }
__device__ __forceinline__ float lanef(float x, int l) { return __uint_as_float(__builtin_amdgcn_readlane(__float_as_uint(x), l)); }
__device__ __forceinline__ unsigned now() { return (unsigned)__builtin_amdgcn_s_memrealtime(); }
template <int I, int N, class F> __device__ __forceinline__ void sfor(F&& f) {
  if constexpr (I < N) { f(std::integral_constant<int, I>{}); sfor<I + 1, N>(f); }
}
template <int N> using ic = std::integral_constant<int, N>;
__device__ __forceinline__ void wg_barrier() {
  asm volatile("s_waitcnt lgkmcnt(0)\n\ts_barrier" ::: "memory");
}
__device__ __forceinline__ unsigned mix(unsigned e) { return (e ^ 0x5bd1e995u) * 0x9E3779B1u + 0x7f4a7c15u; }
constexpr unsigned PENDING = 0x7fc0dead;

__global__ void __launch_bounds__(NTHR, 1) attn_decode(const uint16_t* q_, const uint16_t* k_, const uint16_t* v_,
                                                       __hip_bfloat16* out_, unsigned char* ws_) {
  __shared__ __attribute__((aligned(16))) float lds[L_END];
  u64x4 a03; u64 a4;
  asm volatile("s_load_dwordx8 %0, %2, 0x0\n\ts_load_dwordx2 %1, %2, 0x20" : "=&s"(a03), "=&s"(a4) : "s"(__builtin_amdgcn_kernarg_segment_ptr()));
  const int h = blockIdx.x / NSPLIT, s = blockIdx.x % NSPLIT;
  const int tid = threadIdx.x, lane = tid & 63, wave = uni(tid >> 6), r = lane >> 4, c = lane & 15;
  uint32_t voff = lane * 16, qoff = h * D * 2 + c * 16;
  uint32_t base = h * HEAD_BYTES ^ spread(wave, 8, 2) ^ spread(s, 10, 3) ^ spread(h, 13, 5);
  asm volatile("" : "+v"(voff), "+v"(qoff));
  asm volatile("" : "+s"(base));
  asm volatile("s_waitcnt lgkmcnt(0)" : "+s"(a03), "+s"(a4));
  const unsigned char* const q = (const unsigned char*)a03[0];
  const void *const k = (const void*)a03[1], *const v = (const void*)a03[2];
  void* const out = (void*)a03[3];
  void* const ws = (void*)a4;
  const auto rw = __builtin_amdgcn_make_buffer_rsrc((void*)ws, 0, WS_END, 0x00020000);
  lu32* const lw = (lu32*)lds;

  if (wave == WAVES) {
    unsigned gep_v = __builtin_amdgcn_raw_buffer_load_b32(rw, 0, WS_EPOCH, AUX_SC1);
    const unsigned nominal = now() + SOFF;
    if (lane < 4 * WAVES + 1)
      __hip_atomic_store(lw + L_MX + lane, lane < WAVES ? PENDING : (lane >= 2 * WAVES && lane < 3 * WAVES) || lane == 4 * WAVES ? nominal : 0u, RLX_WG);
    wg_barrier();
    __builtin_amdgcn_sched_barrier(0);
    asm volatile("" : "+v"(gep_v));
    const unsigned rtag = mix(uni(gep_v)) ^ 0x68e31da4u;
    const int pj = blockIdx.x >> 4;
    const uint32_t pub_off = ((int)(blockIdx.x & 15) == (pj & 7) && lane == 0) ? pj * 8 : WS_END;
    int step = 0;
    int ref = 0, gref = -1, drift = 0;
    for (int it = 0; it < 8192; ++it) {
      __builtin_amdgcn_s_sleep(12);
      const unsigned og = __hip_atomic_load(lw + L_ORG + (lane & 7), RLX_WG);
      int gmin = (int)og; gmin = min(gmin, dppi<XOR1>(gmin)); gmin = min(gmin, dppi<XOR2>(gmin));
      const int G = __builtin_amdgcn_readlane(gmin, 4);
      if (G >= GEXIT) break;
      const int gx = X0 + (step >> 1) * XS;
      if (step < 2 * NX && G >= gx + (step & 1) * XD) {
        asm volatile("" ::: "memory");
        if ((step & 1) == 0) {
          int d = (int)(og - nominal); d += dppi<XOR1>(d); d += dppi<XOR2>(d);
          const unsigned mine = nominal + (unsigned)(__builtin_amdgcn_readlane(d, 0) >> 2);
          __builtin_amdgcn_raw_buffer_store_b64(u32x2{mine, rtag}, rw, pub_off, WS_REF + (step >> 1) * 256, AUX_SC1);
        } else {
          const u32x2 smp = __builtin_amdgcn_raw_buffer_load_b64(rw, c * 8, WS_REF + (step >> 1) * 256, AUX_SC1);
          const bool ok = smp[1] == rtag;
          float e = ok ? (float)(int)(smp[0] - nominal) : 0.f, n = ok ? 1.f : 0.f;
          e += dpp<XOR1>(e); e += dpp<XOR2>(e); e += dpp<XOR7>(e); e += dpp<XOR8>(e);
          n += dpp<XOR1>(n); n += dpp<XOR2>(n); n += dpp<XOR7>(n); n += dpp<XOR8>(n);
          if (lanef(n, 0) >= 8.f) {
            const int mean = (int)uni((unsigned)(int)(e * __builtin_amdgcn_rcpf(n)));
            if (gref >= 0) drift = min(max((mean - ref) * 16 / (gx - gref), -DMAX), DMAX);
            ref = mean; gref = gx;
          }
        }
        ++step;
      }
      if (gref >= 0) __hip_atomic_store(lw + L_REF, nominal + (unsigned)(ref + ((G + 1 - gref) * drift >> 4)), RLX_WG);
    }
    return;
  }

  constexpr float C2 = 0.088388347648318440f * 1.4426950408889634f;
  const auto rk = __builtin_amdgcn_make_buffer_rsrc((void*)k, 0, KV_BYTES, 0x00020000);
  const auto rv = __builtin_amdgcn_make_buffer_rsrc((void*)v, 0, KV_BYTES, 0x00020000);
  u32x4 B[16];
  auto ld = [&](decltype(rk) rs, uint32_t a, auto I) {
    B[I.value] = __builtin_amdgcn_raw_buffer_load_b128(rs, voff, a ^ spread(I.value, 0, 4), AUX_NT);
    __builtin_amdgcn_sched_barrier(0);
  };
  int lag;
  auto steer = [&](int G) {
    const unsigned own = now() - (unsigned)(G * SLOPE) / 10u;
    lag = (int)(own - uni(__hip_atomic_load(lw + L_REF, RLX_WG))) - THETA;
    if (G > 0) __hip_atomic_store(lw + L_ORG + wave + (lane & 1) * WAVES, (lane & 1) ? (unsigned)G : own, RLX_WG);
  };
  auto arrived = [&](const u32x4& x, auto THROTTLE) {
    asm volatile("" :: "v"(x) : "memory");
    if constexpr (THROTTLE.value)
      asm volatile("s_cmp_gt_i32 %0, 0\n\ts_cbranch_scc1 1f\n\ts_waitcnt vmcnt(%1)\n1:" :: "s"(lag), "i"(INF - 2) : "scc", "memory");
    __builtin_amdgcn_sched_barrier(0);
  };
  const u32x4 qv = *(const gu32x4*)(q + qoff);
  __builtin_amdgcn_sched_barrier(0);
  sfor<0, INF>([&](auto I) { ld(rk, base, I); });
  const unsigned epoch_v = __builtin_amdgcn_raw_buffer_load_b32(rw, 0, WS_EPOCH + h * 256, AUX_SC1);
  asm volatile("s_barrier" ::: "memory");
  float* const sl = lds + L_SC + wave * (NGRP * 64) + lane;
  float mx = -INFINITY;
  auto kgroup = [&](uint32_t cur, decltype(rk) rn, uint32_t next, int g) {
    float x[4];
    steer(g);
    sfor<0, 16>([&](auto I) {
      constexpr int i = I.value;
      arrived(B[i], std::true_type{});
      if constexpr (i + INF < 16) ld(rk, cur, ic<(i + INF) % 16>{});
      else ld(rn, next, ic<(i + INF) % 16>{});
      float d = 0.f;
#pragma unroll
      for (int j = 0; j < 4; ++j) d = dot2(B[i][j], qv[j], d);
      if constexpr ((i & 1) == 1) d = tr<XOR8, 8>(x[0], d, c);
      if constexpr ((i & 3) == 3) d = tr<XOR7, 4>(x[1], d, c);
      if constexpr ((i & 7) == 7) d = tr<XOR2, 2>(x[2], d, c);
      if constexpr (i == 15) d = tr<XOR1, 1>(x[3], d, c);
      asm volatile("" : "+v"(d) :: "memory");
      x[(i & 1) == 0 ? 0 : (i & 3) == 1 ? 1 : (i & 7) == 3 ? 2 : i == 7 ? 3 : 0] = d;
      __builtin_amdgcn_sched_barrier(0);
    });
    mx = fmaxf(mx, x[0]);
    sl[64 * g] = x[0];
    __builtin_amdgcn_sched_barrier(0);
  };
#pragma unroll 1
  for (int g = 0; g < NGRP - 1; ++g) kgroup(base ^ spread(g, 4, 4), rk, base ^ spread(g + 1, 4, 4), g);
  kgroup(base ^ spread(NGRP - 1, 4, 4), rv, base, NGRP - 1);
  const float m = wave_max(mx) * C2;
  if (lane == 0) __hip_atomic_store(lw + L_MX + wave, __float_as_uint(m), RLX_WG);
  const unsigned epoch = uni(epoch_v), tag = mix(epoch);
  float mwg, fw;
  float l = 0.f;
  f32x2 acc[4];
#pragma unroll
  for (int i = 0; i < 4; ++i) acc[i] = f32x2{0.f, 0.f};
  auto vgroup = [&](uint32_t cur, uint32_t next, int g, auto LAST) {
    constexpr bool last = LAST.value;
    steer(NGRP + g);
    const float p = __builtin_amdgcn_exp2f(fmaf(sl[64 * g], C2, -m));
    l += p;
    sfor<0, 16>([&](auto I) {
      constexpr int i = I.value;
      arrived(B[i], std::integral_constant<bool, !last || i + INF < 16>{});
      if constexpr (i + INF < 16) ld(rv, cur, ic<(i + INF) % 16>{});
      else if constexpr (!last) ld(rv, next, ic<(i + INF) % 16>{});
      if constexpr (last && i == 15 - INF) {
        unsigned u[WAVES];
        for (int spin = 0; spin < (1 << 20); ++spin) {
#pragma unroll
          for (int w = 0; w < WAVES; ++w) u[w] = __hip_atomic_load(lw + L_MX + w, RLX_WG);
          if (uni((u[0] == PENDING) | (u[1] == PENDING) | (u[2] == PENDING) | (u[3] == PENDING)) == 0) break;
        }
        mwg = fmaxf(fmaxf(__uint_as_float(u[0]), __uint_as_float(u[1])), fmaxf(__uint_as_float(u[2]), __uint_as_float(u[3])));
        fw = __builtin_amdgcn_exp2f(m - mwg);
        l = wave_sum(l) * fw;
        if (lane == 0) lds[L_SUM + wave] = l;
        __builtin_amdgcn_sched_barrier(0);
      }
      const float pb = dpp<ROWBCAST + ((i & 1) << 3 | (i & 2) << 1 | (i & 4) >> 1 | (i & 8) >> 3)>(p);
#pragma unroll
      for (int j = 0; j < 4; ++j) {
        const f32x2 vv = {__builtin_bit_cast(float, B[i][j] << 16), __builtin_bit_cast(float, B[i][j] & 0xffff0000u)};
        acc[j] = __builtin_elementwise_fma(f32x2{pb, pb}, vv, acc[j]);
      }
#pragma unroll
      for (int j = 0; j < 4; ++j) asm volatile("" : "+v"(acc[j]) :: "memory");
      __builtin_amdgcn_sched_barrier(0);
    });
  };
#pragma unroll 1
  for (int g = 0; g < NGRP - 1; ++g) vgroup(base ^ spread(g, 4, 4), base ^ spread(g + 1, 4, 4), g, std::false_type{});
  vgroup(base ^ spread(NGRP - 1, 4, 4), 0, NGRP - 1, std::true_type{});
  {
    const float y0 = fold32(acc[0][0], acc[2][0]), y1 = fold32(acc[0][1], acc[2][1]);
    const float y2 = fold32(acc[1][0], acc[3][0]), y3 = fold32(acc[1][1], acc[3][1]);
    *reinterpret_cast<f32x2*>(lds + L_ACC + wave * D + 8 * c + 2 * r) = f32x2{fold16(y0, y2), fold16(y1, y3)} * fw;
  }
  const bool merger = s == NSPLIT - 1;
  u32x4 ga[NSPLIT - 1];
  u32x2 gm;
  auto poll = [&]() {
    asm volatile("" ::: "memory");
    gm = __builtin_amdgcn_raw_buffer_load_b64(rw, c * 8, WS_ML + h * 128, AUX_SC1);
#pragma unroll
    for (int j = 0; j < NSPLIT - 1; ++j)
      ga[j] = __builtin_amdgcn_raw_buffer_load_b128(rw, voff + (j & 3) * 1024, WS_ACC + h * (NSPLIT * 1024) + (j >> 2) * 4096, AUX_SC1);
    asm volatile("" ::: "memory");
    __builtin_amdgcn_sched_barrier(0);
  };
  if (merger && wave == 0) poll();
  wg_barrier();
  if (wave != 0) return;
  f32x2 o = *reinterpret_cast<const f32x2*>(lds + L_ACC + 2 * lane);
#pragma unroll
  for (int w = 1; w < WAVES; ++w) o += *reinterpret_cast<const f32x2*>(lds + L_ACC + w * D + 2 * lane);
  const f32x4 l4 = *reinterpret_cast<const f32x4*>(lds + L_SUM);
  const float L = (l4[0] + l4[1]) + (l4[2] + l4[3]);
  if (!merger) {
    __builtin_amdgcn_raw_buffer_store_b128(u32x4{__float_as_uint(o[0]), tag, __float_as_uint(o[1]), tag}, rw, voff, WS_ACC + blockIdx.x * 1024, AUX_SC1);
    __builtin_amdgcn_raw_buffer_store_b64(u32x2{__float_as_uint(lane ? L : mwg), tag}, rw, lane < 2 ? lane * 64u : WS_END, WS_ML + h * 128 + s * 8, AUX_SC1);
    return;
  }
  for (int spin = 0; spin < (1 << 21); ++spin) {
    unsigned lo = (lane & 7) == NSPLIT - 1 ? tag : gm[1], hi = lo;
#pragma unroll
    for (int j = 0; j < NSPLIT - 1; ++j) { lo = min(lo, min(ga[j][1], ga[j][3])); hi = max(hi, max(ga[j][1], ga[j][3])); }
    if (__all(lo == tag && hi == tag)) break;
    poll();
  }
  __builtin_amdgcn_raw_buffer_store_b32(epoch + 1u, rw, lane == 0 ? 0u : WS_END, WS_EPOCH + h * 256, AUX_SC1);
  __builtin_amdgcn_sched_barrier(0);
  const float ml = (lane & 7) == NSPLIT - 1 ? ((lane & 8) ? L : mwg) : __uint_as_float(gm[0]);
  float M = ml;
  M = fmaxf(M, dpp<XOR1>(M)); M = fmaxf(M, dpp<XOR2>(M)); M = fmaxf(M, dpp<XOR7>(M));
  const float wt = __builtin_amdgcn_exp2f(ml - M);
  float LL = wt * dpp<XOR8>(ml);
  LL += dpp<XOR1>(LL); LL += dpp<XOR2>(LL); LL += dpp<XOR7>(LL);
  const float inv = 1.0f / lanef(LL, 0);
  f32x2 oo = o * lanef(wt, NSPLIT - 1);
#pragma unroll
  for (int j = 0; j < NSPLIT - 1; ++j) {
    const float wj = lanef(wt, j);
    oo = __builtin_elementwise_fma(f32x2{wj, wj}, f32x2{__uint_as_float(ga[j][0]), __uint_as_float(ga[j][2])}, oo);
  }
  oo *= inv;
  const __hip_bfloat16 b0 = __float2bfloat16(oo[0]), b1 = __float2bfloat16(oo[1]);
  __builtin_amdgcn_raw_buffer_store_b32((unsigned)__builtin_bit_cast(uint16_t, b0) | ((unsigned)__builtin_bit_cast(uint16_t, b1) << 16),
                                        __builtin_amdgcn_make_buffer_rsrc((void*)out, 0, H * D * 2, 0x00020000), lane * 4, h * D * 2, AUX_SC1);
}

extern "C" void kernel_launch(void* const* d_in, const int* in_sizes, int n_in, void* d_out, int out_size, void* d_ws,
                              size_t ws_size, hipStream_t stream) {
  attn_decode<<<H * NSPLIT, NTHR, 0, stream>>>((const uint16_t*)d_in[0], (const uint16_t*)d_in[1], (const uint16_t*)d_in[2],
                                               (__hip_bfloat16*)d_out, (unsigned char*)d_ws);
}
```

```cpp
#include <hip/hip_runtime.h>
#include <hip/hip_bf16.h>
#include <cstdint>
#include <type_traits>

constexpr unsigned AM[18] = {0x60, 0x28, 0x211, 0x40, 0x1000, 0x139a, 0x1506, 0x81, 0x32d, 0x3, 0x800, 0x324, 0x1340,
                             0x258, 0x0, 0x0, 0x400, 0x110};
constexpr int SLOPE = 2130;
constexpr int THETA = 40, FAR = 80;
constexpr int INF = 8;
constexpr int SOFF = 120;
constexpr int NX = 3, X0 = 2, XS = 10, XD = 2;
constexpr int GEXIT = 29, DMAX = 160;

constexpr int H = 32, SK = 32768, D = 128, NSPLIT = 8, WAVES = 4, NTHR = (WAVES + 1) * 64, NGRP = 16;
constexpr uint32_t HEAD_BYTES = (uint32_t)SK * D * 2;
constexpr uint32_t KV_BYTES = (uint32_t)H * HEAD_BYTES;
static_assert(NSPLIT * WAVES * NGRP * 16 * 1024 == HEAD_BYTES, "splits x waves x groups x loads cover a head");
constexpr uint32_t spread(uint32_t x, int first, int n) {
  uint32_t a = 0;
  for (int j = 0; j < n; ++j) if (x >> j & 1) a ^= AM[first + j] << 10;
  return a;
}
constexpr bool map_is_bijective() {
  uint32_t v[13] = {};
  for (int i = 0; i < 13; ++i) v[i] = AM[i];
  for (int i = 0; i < 18; ++i) if (AM[i] >> 13) return false;
  for (int b = 0; b < 13; ++b) {
    int p = -1;
    for (int i = b; i < 13 && p < 0; ++i) if (v[i] >> b & 1) p = i;
    if (p < 0) return false;
    const uint32_t t = v[p]; v[p] = v[b]; v[b] = t;
    for (int i = 0; i < 13; ++i) if (i != b && (v[i] >> b & 1)) v[i] ^= v[b];
  }
  return true;
}
static_assert(map_is_bijective(), "");
constexpr uint32_t WS_EPOCH = 0, WS_ML = 16384, WS_REF = 32768, WS_ACC = 65536, WS_END = WS_ACC + H * NSPLIT * 1024;
constexpr int L_SC = 0, L_ACC = WAVES * NGRP * 64, L_MX = L_ACC + WAVES * D, L_SUM = L_MX + WAVES, L_ORG = L_SUM + WAVES,
              L_GRP = L_ORG + WAVES, L_REF = L_GRP + WAVES, L_END = L_REF + 1;

using bf16x2 = __attribute__((ext_vector_type(2))) __bf16;
using u32x2 = __attribute__((ext_vector_type(2))) unsigned;
using u32x4 = __attribute__((ext_vector_type(4))) unsigned;
using f32x4 = __attribute__((ext_vector_type(4))) float;
using f32x2 = __attribute__((ext_vector_type(2))) float;
using u64 = unsigned long long;
using u64x4 = __attribute__((ext_vector_type(4))) u64;
typedef __attribute__((address_space(1))) u32x4 gu32x4;
typedef __attribute__((address_space(3))) unsigned lu32;
#define RLX_WG __ATOMIC_RELAXED, __HIP_MEMORY_SCOPE_WORKGROUP
constexpr int AUX_NT = 2, AUX_SC1 = 16;

__device__ __forceinline__ float dot2(unsigned k, unsigned q, float acc) {
  return __builtin_amdgcn_fdot2_f32_bf16(__builtin_bit_cast(bf16x2, k), __builtin_bit_cast(bf16x2, q), acc, false);
}
template <int CTRL> __device__ __forceinline__ int dppi(int x) { return __builtin_amdgcn_mov_dpp(x, CTRL, 0xf, 0xf, true); }
template <int CTRL> __device__ __forceinline__ float dpp(float x) { return __builtin_bit_cast(float, dppi<CTRL>(__builtin_bit_cast(int, x))); }
constexpr int XOR1 = 0xB1, XOR2 = 0x4E, XOR7 = 0x141, XOR8 = 0x128, ROWBCAST = 0x150;
__device__ __forceinline__ float fold32(float a, float b) {
  auto t = __builtin_amdgcn_permlane32_swap(__float_as_uint(a), __float_as_uint(b), false, false);
  return __uint_as_float(t[0]) + __uint_as_float(t[1]);
}
__device__ __forceinline__ float fold16(float a, float b) {
  auto t = __builtin_amdgcn_permlane16_swap(__float_as_uint(a), __float_as_uint(b), false, false);
  return __uint_as_float(t[0]) + __uint_as_float(t[1]);
}
__device__ __forceinline__ float wave_max(float x) {
  x = fmaxf(x, dpp<XOR1>(x)); x = fmaxf(x, dpp<XOR2>(x)); x = fmaxf(x, dpp<XOR7>(x)); x = fmaxf(x, dpp<XOR8>(x));
  auto s = __builtin_amdgcn_permlane16_swap(__float_as_uint(x), __float_as_uint(x), false, false);
  x = fmaxf(__uint_as_float(s[0]), __uint_as_float(s[1]));
  auto t = __builtin_amdgcn_permlane32_swap(__float_as_uint(x), __float_as_uint(x), false, false);
  return fmaxf(__uint_as_float(t[0]), __uint_as_float(t[1]));
}
__device__ __forceinline__ float wave_sum(float x) {
  x += dpp<XOR1>(x); x += dpp<XOR2>(x); x += dpp<XOR7>(x); x += dpp<XOR8>(x);
  x = fold16(x, x);
  return fold32(x, x);
}
template <int CTRL, int BIT> __device__ __forceinline__ float tr(float a, float b, int c) {
  const float lo = a + dpp<CTRL>(a), hi = b + dpp<CTRL>(b);
  return (c & BIT) ? hi : lo;
}
__device__ __forceinline__ unsigned uni(unsigned x) { return __builtin_amdgcn_readfirstlane(x); }
__device__ __forceinline__ float lanef(float x, int l) { return __uint_as_float(__builtin_amdgcn_readlane(__float_as_uint(x), l)); }
__device__ __forceinline__ unsigned now() { return (unsigned)__builtin_amdgcn_s_memrealtime(); }
template <int I, int N, class F> __device__ __forceinline__ void sfor(F&& f) {
  if constexpr (I < N) { f(std::integral_constant<int, I>{}); sfor<I + 1, N>(f); }
}
template <int N> using ic = std::integral_constant<int, N>;
__device__ __forceinline__ void wg_barrier() {
  asm volatile("s_waitcnt lgkmcnt(0)\n\ts_barrier" ::: "memory");
}
__device__ __forceinline__ unsigned mix(unsigned e) { return (e ^ 0x5bd1e995u) * 0x9E3779B1u + 0x7f4a7c15u; }
constexpr unsigned PENDING = 0x7fc0dead;

__global__ void __launch_bounds__(NTHR, 2) attn_decode(const uint16_t* q_, const uint16_t* k_, const uint16_t* v_,
                                                       __hip_bfloat16* out_, unsigned char* ws_) {
  __shared__ __attribute__((aligned(16))) float lds[L_END];
  u64x4 a03; u64 a4;
  asm volatile("s_load_dwordx8 %0, %2, 0x0\n\ts_load_dwordx2 %1, %2, 0x20" : "=&s"(a03), "=&s"(a4) : "s"(__builtin_amdgcn_kernarg_segment_ptr()));
  const int h = blockIdx.x / NSPLIT, s = blockIdx.x % NSPLIT;
  const int tid = threadIdx.x, lane = tid & 63, wave = uni(tid >> 6), r = lane >> 4, c = lane & 15;
  uint32_t voff = lane * 16, qoff = h * D * 2 + c * 16;
  uint32_t base = h * HEAD_BYTES ^ spread(wave, 8, 2) ^ spread(s, 10, 3) ^ spread(h, 13, 5);
  asm volatile("" : "+v"(voff), "+v"(qoff));
  asm volatile("" : "+s"(base));
  asm volatile("s_waitcnt lgkmcnt(0)" : "+s"(a03), "+s"(a4));
  const unsigned char* const q = (const unsigned char*)a03[0];
  const void *const k = (const void*)a03[1], *const v = (const void*)a03[2];
  void* const out = (void*)a03[3];
  void* const ws = (void*)a4;
  const auto rw = __builtin_amdgcn_make_buffer_rsrc((void*)ws, 0, WS_END, 0x00020000);
  lu32* const lw = (lu32*)lds;

  if (wave == WAVES) {
    unsigned gep_v = __builtin_amdgcn_raw_buffer_load_b32(rw, 0, WS_EPOCH, AUX_SC1);
    const unsigned nominal = now() + SOFF;
    if (lane < 4 * WAVES + 1)
      __hip_atomic_store(lw + L_MX + lane, lane < WAVES ? PENDING : (lane >= 2 * WAVES && lane < 3 * WAVES) || lane == 4 * WAVES ? nominal : 0u, RLX_WG);
    wg_barrier();
    __builtin_amdgcn_sched_barrier(0);
    asm volatile("" : "+v"(gep_v));
    const unsigned rtag = mix(uni(gep_v)) ^ 0x68e31da4u;
    const int pj = blockIdx.x >> 4;
    const uint32_t pub_off = ((int)(blockIdx.x & 15) == (pj & 7) && lane == 0) ? pj * 8 : WS_END;
    int step = 0;
    int ref = 0, gref = -1, drift = 0;
    for (int it = 0; it < 8192; ++it) {
      __builtin_amdgcn_s_sleep(12);
      const unsigned og = __hip_atomic_load(lw + L_ORG + (lane & 7), RLX_WG);
      int gmin = (int)og; gmin = min(gmin, dppi<XOR1>(gmin)); gmin = min(gmin, dppi<XOR2>(gmin));
      const int G = __builtin_amdgcn_readlane(gmin, 4);
      if (G >= GEXIT) break;
      const int gx = X0 + (step >> 1) * XS;
      if (step < 2 * NX && G >= gx + (step & 1) * XD) {
        asm volatile("" ::: "memory");
        if ((step & 1) == 0) {
          int d = (int)(og - nominal); d += dppi<XOR1>(d); d += dppi<XOR2>(d);
          const unsigned mine = nominal + (unsigned)(__builtin_amdgcn_readlane(d, 0) >> 2);
          __builtin_amdgcn_raw_buffer_store_b64(u32x2{mine, rtag}, rw, pub_off, WS_REF + (step >> 1) * 256, AUX_SC1);
        } else {
          const u32x2 smp = __builtin_amdgcn_raw_buffer_load_b64(rw, c * 8, WS_REF + (step >> 1) * 256, AUX_SC1);
          const bool ok = smp[1] == rtag;
          float e = ok ? (float)(int)(smp[0] - nominal) : 0.f, n = ok ? 1.f : 0.f;
          e += dpp<XOR1>(e); e += dpp<XOR2>(e); e += dpp<XOR7>(e); e += dpp<XOR8>(e);
          n += dpp<XOR1>(n); n += dpp<XOR2>(n); n += dpp<XOR7>(n); n += dpp<XOR8>(n);
          if (lanef(n, 0) >= 8.f) {
            const int mean = (int)uni((unsigned)(int)(e * __builtin_amdgcn_rcpf(n)));
            if (gref >= 0) drift = min(max((mean - ref) * 16 / (gx - gref), -DMAX), DMAX);
            ref = mean; gref = gx;
          }
        }
        ++step;
      }
      if (gref >= 0) __hip_atomic_store(lw + L_REF, nominal + (unsigned)(ref + ((G + 1 - gref) * drift >> 4)), RLX_WG);
    }
    return;
  }

  constexpr float C2 = 0.088388347648318440f * 1.4426950408889634f;
  const auto rk = __builtin_amdgcn_make_buffer_rsrc((void*)k, 0, KV_BYTES, 0x00020000);
  const auto rv = __builtin_amdgcn_make_buffer_rsrc((void*)v, 0, KV_BYTES, 0x00020000);
  u32x4 B[16];
  auto ld = [&](decltype(rk) rs, uint32_t a, auto I) {
    B[I.value] = __builtin_amdgcn_raw_buffer_load_b128(rs, voff, a ^ spread(I.value, 0, 4), AUX_NT);
    __builtin_amdgcn_sched_barrier(0);
  };
  int lag;
  auto steer = [&](int G) {
    const unsigned own = now() - (unsigned)(G * SLOPE) / 10u;
    lag = (int)(own - uni(__hip_atomic_load(lw + L_REF, RLX_WG))) - THETA;
    if (G > 0) __hip_atomic_store(lw + L_ORG + wave + (lane & 1) * WAVES, (lane & 1) ? (unsigned)G : own, RLX_WG);
  };
  auto arrived = [&](const u32x4& x, auto THROTTLE) {
    asm volatile("" :: "v"(x) : "memory");
    if constexpr (THROTTLE.value)
      asm volatile("s_cmp_gt_i32 %0, %1\n\ts_cbranch_scc1 1f\n\ts_waitcnt vmcnt(5)\n\t"
                   "s_cmp_gt_i32 %0, 0\n\ts_cbranch_scc1 1f\n\ts_waitcnt vmcnt(4)\n1:" :: "s"(lag), "i"(FAR) : "scc", "memory");
    __builtin_amdgcn_sched_barrier(0);
  };
  const u32x4 qv = *(const gu32x4*)(q + qoff);
  __builtin_amdgcn_sched_barrier(0);
  sfor<0, INF>([&](auto I) { ld(rk, base, I); });
  const unsigned epoch_v = __builtin_amdgcn_raw_buffer_load_b32(rw, 0, WS_EPOCH + h * 256, AUX_SC1);
  asm volatile("s_barrier" ::: "memory");
  float* const sl = lds + L_SC + wave * (NGRP * 64) + lane;
  float mx = -INFINITY;
  auto kgroup = [&](uint32_t cur, decltype(rk) rn, uint32_t next, int g) {
    float x[4];
    steer(g);
    sfor<0, 16>([&](auto I) {
      constexpr int i = I.value;
      arrived(B[i], std::true_type{});
      if constexpr (i + INF < 16) ld(rk, cur, ic<(i + INF) % 16>{});
      else ld(rn, next, ic<(i + INF) % 16>{});
      float d = 0.f;
#pragma unroll
      for (int j = 0; j < 4; ++j) d = dot2(B[i][j], qv[j], d);
      if constexpr ((i & 1) == 1) d = tr<XOR8, 8>(x[0], d, c);
      if constexpr ((i & 3) == 3) d = tr<XOR7, 4>(x[1], d, c);
      if constexpr ((i & 7) == 7) d = tr<XOR2, 2>(x[2], d, c);
      if constexpr (i == 15) d = tr<XOR1, 1>(x[3], d, c);
      asm volatile("" : "+v"(d) :: "memory");
      x[(i & 1) == 0 ? 0 : (i & 3) == 1 ? 1 : (i & 7) == 3 ? 2 : i == 7 ? 3 : 0] = d;
      __builtin_amdgcn_sched_barrier(0);
    });
    mx = fmaxf(mx, x[0]);
    sl[64 * g] = x[0];
    __builtin_amdgcn_sched_barrier(0);
  };
#pragma unroll 1
  for (int g = 0; g < NGRP - 1; ++g) kgroup(base ^ spread(g, 4, 4), rk, base ^ spread(g + 1, 4, 4), g);
  kgroup(base ^ spread(NGRP - 1, 4, 4), rv, base, NGRP - 1);
  const float m = wave_max(mx);
  if (lane == 0) __hip_atomic_store(lw + L_MX + wave, __float_as_uint(m), RLX_WG);
  const unsigned epoch = uni(epoch_v), tag = mix(epoch);
  float mwg, fw;
  float l = 0.f;
  f32x2 acc[4];
#pragma unroll
  for (int i = 0; i < 4; ++i) acc[i] = f32x2{0.f, 0.f};
  auto vgroup = [&](uint32_t cur, uint32_t next, int g, auto LAST) {
    constexpr bool last = LAST.value;
    steer(NGRP + g);
    const float p = __builtin_amdgcn_exp2f((sl[64 * g] - m) * C2);
    l += p;
    sfor<0, 16>([&](auto I) {
      constexpr int i = I.value;
      arrived(B[i], std::integral_constant<bool, !last || i + INF < 16>{});
      if constexpr (i + INF < 16) ld(rv, cur, ic<(i + INF) % 16>{});
      else if constexpr (!last) ld(rv, next, ic<(i + INF) % 16>{});
      if constexpr (last && i == 15 - INF) {
        unsigned u[WAVES];
        for (int spin = 0; spin < (1 << 20); ++spin) {
#pragma unroll
          for (int w = 0; w < WAVES; ++w) u[w] = __hip_atomic_load(lw + L_MX + w, RLX_WG);
          if (uni((u[0] == PENDING) | (u[1] == PENDING) | (u[2] == PENDING) | (u[3] == PENDING)) == 0) break;
        }
        mwg = fmaxf(fmaxf(__uint_as_float(u[0]), __uint_as_float(u[1])), fmaxf(__uint_as_float(u[2]), __uint_as_float(u[3])));
        fw = __builtin_amdgcn_exp2f((m - mwg) * C2);
        l = wave_sum(l) * fw;
        if (lane == 0) lds[L_SUM + wave] = l;
        __builtin_amdgcn_sched_barrier(0);
      }
      const float pb = dpp<ROWBCAST + ((i & 1) << 3 | (i & 2) << 1 | (i & 4) >> 1 | (i & 8) >> 3)>(p);
#pragma unroll
      for (int j = 0; j < 4; ++j) {
        const f32x2 vv = {__builtin_bit_cast(float, B[i][j] << 16), __builtin_bit_cast(float, B[i][j] & 0xffff0000u)};
        acc[j] = __builtin_elementwise_fma(f32x2{pb, pb}, vv, acc[j]);
      }
#pragma unroll
      for (int j = 0; j < 4; ++j) asm volatile("" : "+v"(acc[j]) :: "memory");
      __builtin_amdgcn_sched_barrier(0);
    });
  };
#pragma unroll 1
  for (int g = 0; g < NGRP - 1; ++g) vgroup(base ^ spread(g, 4, 4), base ^ spread(g + 1, 4, 4), g, std::false_type{});
  vgroup(base ^ spread(NGRP - 1, 4, 4), 0, NGRP - 1, std::true_type{});
  {
    const float y0 = fold32(acc[0][0], acc[2][0]), y1 = fold32(acc[0][1], acc[2][1]);
    const float y2 = fold32(acc[1][0], acc[3][0]), y3 = fold32(acc[1][1], acc[3][1]);
    *reinterpret_cast<f32x2*>(lds + L_ACC + wave * D + 8 * c + 2 * r) = f32x2{fold16(y0, y2), fold16(y1, y3)} * fw;
  }
  const bool merger = s == NSPLIT - 1;
  u32x4 ga[NSPLIT - 1];
  u32x2 gm;
  auto poll = [&]() {
    asm volatile("" ::: "memory");
    gm = __builtin_amdgcn_raw_buffer_load_b64(rw, c * 8, WS_ML + h * 128, AUX_SC1);
#pragma unroll
    for (int j = 0; j < NSPLIT - 1; ++j)
      ga[j] = __builtin_amdgcn_raw_buffer_load_b128(rw, voff + (j & 3) * 1024, WS_ACC + h * (NSPLIT * 1024) + (j >> 2) * 4096, AUX_SC1);
    asm volatile("" ::: "memory");
    __builtin_amdgcn_sched_barrier(0);
  };
  if (merger && wave == 0) poll();
  wg_barrier();
  if (wave != 0) return;
  f32x2 o = *reinterpret_cast<const f32x2*>(lds + L_ACC + 2 * lane);
#pragma unroll
  for (int w = 1; w < WAVES; ++w) o += *reinterpret_cast<const f32x2*>(lds + L_ACC + w * D + 2 * lane);
  const f32x4 l4 = *reinterpret_cast<const f32x4*>(lds + L_SUM);
  const float L = (l4[0] + l4[1]) + (l4[2] + l4[3]);
  if (!merger) {
    __builtin_amdgcn_raw_buffer_store_b128(u32x4{__float_as_uint(o[0]), tag, __float_as_uint(o[1]), tag}, rw, voff, WS_ACC + blockIdx.x * 1024, AUX_SC1);
    __builtin_amdgcn_raw_buffer_store_b64(u32x2{__float_as_uint(lane ? L : mwg), tag}, rw, lane < 2 ? lane * 64u : WS_END, WS_ML + h * 128 + s * 8, AUX_SC1);
    return;
  }
  for (int spin = 0; spin < (1 << 21); ++spin) {
    unsigned lo = (lane & 7) == NSPLIT - 1 ? tag : gm[1], hi = lo;
#pragma unroll
    for (int j = 0; j < NSPLIT - 1; ++j) { lo = min(lo, min(ga[j][1], ga[j][3])); hi = max(hi, max(ga[j][1], ga[j][3])); }
    if (__all(lo == tag && hi == tag)) break;
    poll();
  }
  __builtin_amdgcn_raw_buffer_store_b32(epoch + 1u, rw, lane == 0 ? 0u : WS_END, WS_EPOCH + h * 256, AUX_SC1);
  __builtin_amdgcn_sched_barrier(0);
  const float ml = (lane & 7) == NSPLIT - 1 ? ((lane & 8) ? L : mwg) : __uint_as_float(gm[0]);
  float M = ml;
  M = fmaxf(M, dpp<XOR1>(M)); M = fmaxf(M, dpp<XOR2>(M)); M = fmaxf(M, dpp<XOR7>(M));
  const float wt = __builtin_amdgcn_exp2f((ml - M) * C2);
  float LL = wt * dpp<XOR8>(ml);
  LL += dpp<XOR1>(LL); LL += dpp<XOR2>(LL); LL += dpp<XOR7>(LL);
  const float inv = 1.0f / lanef(LL, 0);
  f32x2 oo = o * lanef(wt, NSPLIT - 1);
#pragma unroll
  for (int j = 0; j < NSPLIT - 1; ++j) {
    const float wj = lanef(wt, j);
    oo = __builtin_elementwise_fma(f32x2{wj, wj}, f32x2{__uint_as_float(ga[j][0]), __uint_as_float(ga[j][2])}, oo);
  }
  oo *= inv;
  const __hip_bfloat16 b0 = __float2bfloat16(oo[0]), b1 = __float2bfloat16(oo[1]);
  __builtin_amdgcn_raw_buffer_store_b32((unsigned)__builtin_bit_cast(uint16_t, b0) | ((unsigned)__builtin_bit_cast(uint16_t, b1) << 16),
                                        __builtin_amdgcn_make_buffer_rsrc((void*)out, 0, H * D * 2, 0x00020000), lane * 4, h * D * 2, AUX_SC1);
}

extern "C" void kernel_launch(void* const* d_in, const int* in_sizes, int n_in, void* d_out, int out_size, void* d_ws,
                              size_t ws_size, hipStream_t stream) {
  attn_decode<<<H * NSPLIT, NTHR, 0, stream>>>((const uint16_t*)d_in[0], (const uint16_t*)d_in[1], (const uint16_t*)d_in[2],
                                               (__hip_bfloat16*)d_out, (unsigned char*)d_ws);
}
```

```cpp
#include <hip/hip_runtime.h>
#include <hip/hip_bf16.h>
#include <cstdint>
#include <type_traits>

constexpr unsigned AM[18] = {0x60, 0x28, 0x211, 0x40, 0x1000, 0x139a, 0x1506, 0x81, 0x32d, 0x3, 0x800, 0x324, 0x1340,
                             0x258, 0x0, 0x0, 0x400, 0x110};
constexpr int SLOPE = 2130;
constexpr int THETA = 30;
constexpr int INF = 6;
constexpr int SOFF = 120;
constexpr int NX = 3, X0 = 2, XS = 10, XD = 2;
constexpr int GEXIT = 29, DMAX = 160;

constexpr int H = 32, SK = 32768, D = 128, NSPLIT = 8, WAVES = 4, NTHR = (WAVES + 1) * 64, NGRP = 16;
constexpr uint32_t HEAD_BYTES = (uint32_t)SK * D * 2;
constexpr uint32_t KV_BYTES = (uint32_t)H * HEAD_BYTES;
static_assert(NSPLIT * WAVES * NGRP * 16 * 1024 == HEAD_BYTES, "splits x waves x groups x loads cover a head");
constexpr uint32_t spread(uint32_t x, int first, int n) {
  uint32_t a = 0;
  for (int j = 0; j < n; ++j) if (x >> j & 1) a ^= AM[first + j] << 10;
  return a;
}
constexpr bool map_is_bijective() {
  uint32_t v[13] = {};
  for (int i = 0; i < 13; ++i) v[i] = AM[i];
  for (int i = 0; i < 18; ++i) if (AM[i] >> 13) return false;
  for (int b = 0; b < 13; ++b) {
    int p = -1;
    for (int i = b; i < 13 && p < 0; ++i) if (v[i] >> b & 1) p = i;
    if (p < 0) return false;
    const uint32_t t = v[p]; v[p] = v[b]; v[b] = t;
    for (int i = 0; i < 13; ++i) if (i != b && (v[i] >> b & 1)) v[i] ^= v[b];
  }
  return true;
}
static_assert(map_is_bijective(), "");
constexpr uint32_t WS_EPOCH = 0, WS_ML = 16384, WS_REF = 32768, WS_ACC = 65536, WS_END = WS_ACC + H * NSPLIT * 1024;
constexpr int L_SC = 0, L_ACC = WAVES * NGRP * 64, L_MX = L_ACC + WAVES * D, L_SUM = L_MX + WAVES, L_ORG = L_SUM + WAVES,
              L_GRP = L_ORG + WAVES, L_REF = L_GRP + WAVES, L_END = L_REF + 1;

using bf16x2 = __attribute__((ext_vector_type(2))) __bf16;
using u32x2 = __attribute__((ext_vector_type(2))) unsigned;
using u32x4 = __attribute__((ext_vector_type(4))) unsigned;
using f32x4 = __attribute__((ext_vector_type(4))) float;
using f32x2 = __attribute__((ext_vector_type(2))) float;
using u64 = unsigned long long;
using u64x4 = __attribute__((ext_vector_type(4))) u64;
typedef __attribute__((address_space(1))) u32x4 gu32x4;
typedef __attribute__((address_space(3))) unsigned lu32;
#define RLX_WG __ATOMIC_RELAXED, __HIP_MEMORY_SCOPE_WORKGROUP
constexpr int AUX_NT = 2, AUX_SC1 = 16;

__device__ __forceinline__ float dot2(unsigned k, unsigned q, float acc) {
  return __builtin_amdgcn_fdot2_f32_bf16(__builtin_bit_cast(bf16x2, k), __builtin_bit_cast(bf16x2, q), acc, false);
}
template <int CTRL> __device__ __forceinline__ int dppi(int x) { return __builtin_amdgcn_mov_dpp(x, CTRL, 0xf, 0xf, true); }
template <int CTRL> __device__ __forceinline__ float dpp(float x) { return __builtin_bit_cast(float, dppi<CTRL>(__builtin_bit_cast(int, x))); }
constexpr int XOR1 = 0xB1, XOR2 = 0x4E, XOR7 = 0x141, XOR8 = 0x128, ROWBCAST = 0x150;
__device__ __forceinline__ float fold32(float a, float b) {
  auto t = __builtin_amdgcn_permlane32_swap(__float_as_uint(a), __float_as_uint(b), false, false);
  return __uint_as_float(t[0]) + __uint_as_float(t[1]);
}
__device__ __forceinline__ float fold16(float a, float b) {
  auto t = __builtin_amdgcn_permlane16_swap(__float_as_uint(a), __float_as_uint(b), false, false);
  return __uint_as_float(t[0]) + __uint_as_float(t[1]);
}
__device__ __forceinline__ float wave_max(float x) {
  x = fmaxf(x, dpp<XOR1>(x)); x = fmaxf(x, dpp<XOR2>(x)); x = fmaxf(x, dpp<XOR7>(x)); x = fmaxf(x, dpp<XOR8>(x));
  auto s = __builtin_amdgcn_permlane16_swap(__float_as_uint(x), __float_as_uint(x), false, false);
  x = fmaxf(__uint_as_float(s[0]), __uint_as_float(s[1]));
  auto t = __builtin_amdgcn_permlane32_swap(__float_as_uint(x), __float_as_uint(x), false, false);
  return fmaxf(__uint_as_float(t[0]), __uint_as_float(t[1]));
}
__device__ __forceinline__ float wave_sum(float x) {
  x += dpp<XOR1>(x); x += dpp<XOR2>(x); x += dpp<XOR7>(x); x += dpp<XOR8>(x);
  x = fold16(x, x);
  return fold32(x, x);
}
template <int CTRL, int BIT> __device__ __forceinline__ float tr(float a, float b, int c) {
  const float lo = a + dpp<CTRL>(a), hi = b + dpp<CTRL>(b);
  return (c & BIT) ? hi : lo;
}
__device__ __forceinline__ unsigned uni(unsigned x) { return __builtin_amdgcn_readfirstlane(x); }
__device__ __forceinline__ float lanef(float x, int l) { return __uint_as_float(__builtin_amdgcn_readlane(__float_as_uint(x), l)); }
__device__ __forceinline__ unsigned now() { return (unsigned)__builtin_amdgcn_s_memrealtime(); }
template <int I, int N, class F> __device__ __forceinline__ void sfor(F&& f) {
  if constexpr (I < N) { f(std::integral_constant<int, I>{}); sfor<I + 1, N>(f); }
}
template <int N> using ic = std::integral_constant<int, N>;
__device__ __forceinline__ void wg_barrier() {
  asm volatile("s_waitcnt lgkmcnt(0)\n\ts_barrier" ::: "memory");
}
__device__ __forceinline__ unsigned mix(unsigned e) { return (e ^ 0x5bd1e995u) * 0x9E3779B1u + 0x7f4a7c15u; }
constexpr unsigned PENDING = 0x7fc0dead;

__global__ void __launch_bounds__(NTHR, 2) attn_decode(const uint16_t* q_, const uint16_t* k_, const uint16_t* v_,
                                                       __hip_bfloat16* out_, unsigned char* ws_) {
  __shared__ __attribute__((aligned(16))) float lds[L_END];
  u64x4 a03; u64 a4;
  asm volatile("s_load_dwordx8 %0, %2, 0x0\n\ts_load_dwordx2 %1, %2, 0x20" : "=&s"(a03), "=&s"(a4) : "s"(__builtin_amdgcn_kernarg_segment_ptr()));
  const int h = blockIdx.x / NSPLIT, s = blockIdx.x % NSPLIT;
  const int tid = threadIdx.x, lane = tid & 63, wave = uni(tid >> 6), r = lane >> 4, c = lane & 15;
  uint32_t voff = lane * 16, qoff = h * D * 2 + c * 16;
  uint32_t base = h * HEAD_BYTES ^ spread(wave, 8, 2) ^ spread(s, 10, 3) ^ spread(h, 13, 5);
  asm volatile("" : "+v"(voff), "+v"(qoff));
  asm volatile("" : "+s"(base));
  asm volatile("s_waitcnt lgkmcnt(0)" : "+s"(a03), "+s"(a4));
  const unsigned char* const q = (const unsigned char*)a03[0];
  const void *const k = (const void*)a03[1], *const v = (const void*)a03[2];
  void* const out = (void*)a03[3];
  void* const ws = (void*)a4;
  const auto rw = __builtin_amdgcn_make_buffer_rsrc((void*)ws, 0, WS_END, 0x00020000);
  lu32* const lw = (lu32*)lds;

  if (wave == WAVES) {
    unsigned gep_v = __builtin_amdgcn_raw_buffer_load_b32(rw, 0, WS_EPOCH, AUX_SC1);
    const unsigned nominal = now() + SOFF;
    if (lane < 4 * WAVES + 1)
      __hip_atomic_store(lw + L_MX + lane, lane < WAVES ? PENDING : (lane >= 2 * WAVES && lane < 3 * WAVES) || lane == 4 * WAVES ? nominal : 0u, RLX_WG);
    wg_barrier();
    __builtin_amdgcn_sched_barrier(0);
    asm volatile("" : "+v"(gep_v));
    const unsigned rtag = mix(uni(gep_v)) ^ 0x68e31da4u;
    const int pj = blockIdx.x >> 4;
    const uint32_t pub_off = ((int)(blockIdx.x & 15) == (pj & 7) && lane == 0) ? pj * 8 : WS_END;
    int step = 0;
    int ref = 0, gref = -1, drift = 0;
    for (int it = 0; it < 8192; ++it) {
      __builtin_amdgcn_s_sleep(12);
      const unsigned og = __hip_atomic_load(lw + L_ORG + (lane & 7), RLX_WG);
      int gmin = (int)og; gmin = min(gmin, dppi<XOR1>(gmin)); gmin = min(gmin, dppi<XOR2>(gmin));
      const int G = __builtin_amdgcn_readlane(gmin, 4);
      if (G >= GEXIT) break;
      const int gx = X0 + (step >> 1) * XS;
      if (step < 2 * NX && G >= gx + (step & 1) * XD) {
        asm volatile("" ::: "memory");
        if ((step & 1) == 0) {
          int d = (int)(og - nominal); d += dppi<XOR1>(d); d += dppi<XOR2>(d);
          const unsigned mine = nominal + (unsigned)(__builtin_amdgcn_readlane(d, 0) >> 2);
          __builtin_amdgcn_raw_buffer_store_b64(u32x2{mine, rtag}, rw, pub_off, WS_REF + (step >> 1) * 256, AUX_SC1);
        } else {
          const u32x2 smp = __builtin_amdgcn_raw_buffer_load_b64(rw, c * 8, WS_REF + (step >> 1) * 256, AUX_SC1);
          const bool ok = smp[1] == rtag;
          float e = ok ? (float)(int)(smp[0] - nominal) : 0.f, n = ok ? 1.f : 0.f;
          e += dpp<XOR1>(e); e += dpp<XOR2>(e); e += dpp<XOR7>(e); e += dpp<XOR8>(e);
          n += dpp<XOR1>(n); n += dpp<XOR2>(n); n += dpp<XOR7>(n); n += dpp<XOR8>(n);
          if (lanef(n, 0) >= 8.f) {
            const int mean = (int)uni((unsigned)(int)(e * __builtin_amdgcn_rcpf(n)));
            if (gref >= 0) drift = min(max((mean - ref) * 16 / (gx - gref), -DMAX), DMAX);
            ref = mean; gref = gx;
          }
        }
        ++step;
      }
      if (gref >= 0) __hip_atomic_store(lw + L_REF, nominal + (unsigned)(ref + ((G + 1 - gref) * drift >> 4)), RLX_WG);
    }
    return;
  }

  constexpr float C2 = 0.088388347648318440f * 1.4426950408889634f;
  const auto rk = __builtin_amdgcn_make_buffer_rsrc((void*)k, 0, KV_BYTES, 0x00020000);
  const auto rv = __builtin_amdgcn_make_buffer_rsrc((void*)v, 0, KV_BYTES, 0x00020000);
  u32x4 B[16];
  auto ld = [&](decltype(rk) rs, uint32_t a, auto I) {
    B[I.value] = __builtin_amdgcn_raw_buffer_load_b128(rs, voff, a ^ spread(I.value, 0, 4), AUX_NT);
    __builtin_amdgcn_sched_barrier(0);
  };
  int lag;
  auto steer = [&](int G) {
    const unsigned own = now() - (unsigned)(G * SLOPE) / 10u;
    lag = (int)(own - uni(__hip_atomic_load(lw + L_REF, RLX_WG))) - THETA;
    if (G > 0) __hip_atomic_store(lw + L_ORG + wave + (lane & 1) * WAVES, (lane & 1) ? (unsigned)G : own, RLX_WG);
  };
  auto arrived = [&](const u32x4& x, auto THROTTLE) {
    asm volatile("" :: "v"(x) : "memory");
    if constexpr (THROTTLE.value)
      asm volatile("s_cmp_gt_i32 %0, 0\n\ts_cbranch_scc1 1f\n\ts_waitcnt vmcnt(%1)\n1:" :: "s"(lag), "i"(INF - 2) : "scc", "memory");
    __builtin_amdgcn_sched_barrier(0);
  };
  const u32x4 qv = *(const gu32x4*)(q + qoff);
  __builtin_amdgcn_sched_barrier(0);
  sfor<0, INF>([&](auto I) { ld(rk, base, I); });
  const unsigned epoch_v = __builtin_amdgcn_raw_buffer_load_b32(rw, 0, WS_EPOCH + h * 256, AUX_SC1);
  asm volatile("s_barrier" ::: "memory");
  float* const sl = lds + L_SC + wave * (NGRP * 64) + lane;
  float mx = -INFINITY;
  auto kgroup = [&](uint32_t cur, decltype(rk) rn, uint32_t next, int g) {
    float x[4];
    steer(g);
    sfor<0, 16>([&](auto I) {
      constexpr int i = I.value;
      arrived(B[i], std::true_type{});
      if constexpr (i + INF < 16) ld(rk, cur, ic<(i + INF) % 16>{});
      else ld(rn, next, ic<(i + INF) % 16>{});
      float d = 0.f;
#pragma unroll
      for (int j = 0; j < 4; ++j) d = dot2(B[i][j], qv[j], d);
      if constexpr ((i & 1) == 1) d = tr<XOR8, 8>(x[0], d, c);
      if constexpr ((i & 3) == 3) d = tr<XOR7, 4>(x[1], d, c);
      if constexpr ((i & 7) == 7) d = tr<XOR2, 2>(x[2], d, c);
      if constexpr (i == 15) d = tr<XOR1, 1>(x[3], d, c);
      asm volatile("" : "+v"(d) :: "memory");
      x[(i & 1) == 0 ? 0 : (i & 3) == 1 ? 1 : (i & 7) == 3 ? 2 : i == 7 ? 3 : 0] = d;
      __builtin_amdgcn_sched_barrier(0);
    });
    mx = fmaxf(mx, x[0]);
    sl[64 * g] = x[0];
    __builtin_amdgcn_sched_barrier(0);
  };
#pragma unroll 1
  for (int g = 0; g < NGRP - 1; ++g) kgroup(base ^ spread(g, 4, 4), rk, base ^ spread(g + 1, 4, 4), g);
  kgroup(base ^ spread(NGRP - 1, 4, 4), rv, base, NGRP - 1);
  const float m = wave_max(mx);
  if (lane == 0) __hip_atomic_store(lw + L_MX + wave, __float_as_uint(m), RLX_WG);
  const unsigned epoch = uni(epoch_v), tag = mix(epoch);
  float mwg, fw;
  float l = 0.f;
  f32x2 acc[4];
#pragma unroll
  for (int i = 0; i < 4; ++i) acc[i] = f32x2{0.f, 0.f};
  auto vgroup = [&](uint32_t cur, uint32_t next, int g, auto LAST) {
    constexpr bool last = LAST.value;
    steer(NGRP + g);
    const float p = __builtin_amdgcn_exp2f((sl[64 * g] - m) * C2);
    l += p;
    sfor<0, 16>([&](auto I) {
      constexpr int i = I.value;
      arrived(B[i], std::integral_constant<bool, !last || i + INF < 16>{});
      if constexpr (i + INF < 16) ld(rv, cur, ic<(i + INF) % 16>{});
      else if constexpr (!last) ld(rv, next, ic<(i + INF) % 16>{});
      if constexpr (last && i == 15 - INF) {
        unsigned u[WAVES];
        for (int spin = 0; spin < (1 << 20); ++spin) {
#pragma unroll
          for (int w = 0; w < WAVES; ++w) u[w] = __hip_atomic_load(lw + L_MX + w, RLX_WG);
          if (uni((u[0] == PENDING) | (u[1] == PENDING) | (u[2] == PENDING) | (u[3] == PENDING)) == 0) break;
        }
        mwg = fmaxf(fmaxf(__uint_as_float(u[0]), __uint_as_float(u[1])), fmaxf(__uint_as_float(u[2]), __uint_as_float(u[3])));
        fw = __builtin_amdgcn_exp2f((m - mwg) * C2);
        l = wave_sum(l) * fw;
        if (lane == 0) lds[L_SUM + wave] = l;
        __builtin_amdgcn_sched_barrier(0);
      }
      const float pb = dpp<ROWBCAST + ((i & 1) << 3 | (i & 2) << 1 | (i & 4) >> 1 | (i & 8) >> 3)>(p);
#pragma unroll
      for (int j = 0; j < 4; ++j) {
        const f32x2 vv = {__builtin_bit_cast(float, B[i][j] << 16), __builtin_bit_cast(float, B[i][j] & 0xffff0000u)};
        acc[j] = __builtin_elementwise_fma(f32x2{pb, pb}, vv, acc[j]);
      }
#pragma unroll
      for (int j = 0; j < 4; ++j) asm volatile("" : "+v"(acc[j]) :: "memory");
      __builtin_amdgcn_sched_barrier(0);
    });
  };
#pragma unroll 1
  for (int g = 0; g < NGRP - 1; ++g) vgroup(base ^ spread(g, 4, 4), base ^ spread(g + 1, 4, 4), g, std::false_type{});
  vgroup(base ^ spread(NGRP - 1, 4, 4), 0, NGRP - 1, std::true_type{});
  {
    const float y0 = fold32(acc[0][0], acc[2][0]), y1 = fold32(acc[0][1], acc[2][1]);
    const float y2 = fold32(acc[1][0], acc[3][0]), y3 = fold32(acc[1][1], acc[3][1]);
    *reinterpret_cast<f32x2*>(lds + L_ACC + wave * D + 8 * c + 2 * r) = f32x2{fold16(y0, y2), fold16(y1, y3)} * fw;
  }
  const bool merger = s == NSPLIT - 1;
  u32x4 ga[NSPLIT - 1];
  u32x2 gm;
  auto poll = [&]() {
    asm volatile("" ::: "memory");
    gm = __builtin_amdgcn_raw_buffer_load_b64(rw, c * 8, WS_ML + h * 128, AUX_SC1);
#pragma unroll
    for (int j = 0; j < NSPLIT - 1; ++j)
      ga[j] = __builtin_amdgcn_raw_buffer_load_b128(rw, voff + (j & 3) * 1024, WS_ACC + h * (NSPLIT * 1024) + (j >> 2) * 4096, AUX_SC1);
    asm volatile("" ::: "memory");
    __builtin_amdgcn_sched_barrier(0);
  };
  if (merger && wave == 0) poll();
  wg_barrier();
  if (wave != 0) return;
  f32x2 o = *reinterpret_cast<const f32x2*>(lds + L_ACC + 2 * lane);
#pragma unroll
  for (int w = 1; w < WAVES; ++w) o += *reinterpret_cast<const f32x2*>(lds + L_ACC + w * D + 2 * lane);
  const f32x4 l4 = *reinterpret_cast<const f32x4*>(lds + L_SUM);
  const float L = (l4[0] + l4[1]) + (l4[2] + l4[3]);
  if (!merger) {
    __builtin_amdgcn_raw_buffer_store_b128(u32x4{__float_as_uint(o[0]), tag, __float_as_uint(o[1]), tag}, rw, voff, WS_ACC + blockIdx.x * 1024, AUX_SC1);
    __builtin_amdgcn_raw_buffer_store_b64(u32x2{__float_as_uint(lane ? L : mwg), tag}, rw, lane < 2 ? lane * 64u : WS_END, WS_ML + h * 128 + s * 8, AUX_SC1);
    return;
  }
  for (int spin = 0; spin < (1 << 21); ++spin) {
    unsigned lo = (lane & 7) == NSPLIT - 1 ? tag : gm[1], hi = lo;
#pragma unroll
    for (int j = 0; j < NSPLIT - 1; ++j) { lo = min(lo, min(ga[j][1], ga[j][3])); hi = max(hi, max(ga[j][1], ga[j][3])); }
    if (__all(lo == tag && hi == tag)) break;
    poll();
  }
  __builtin_amdgcn_raw_buffer_store_b32(epoch + 1u, rw, lane == 0 ? 0u : WS_END, WS_EPOCH + h * 256, AUX_SC1);
  __builtin_amdgcn_sched_barrier(0);
  const float ml = (lane & 7) == NSPLIT - 1 ? ((lane & 8) ? L : mwg) : __uint_as_float(gm[0]);
  float M = ml;
  M = fmaxf(M, dpp<XOR1>(M)); M = fmaxf(M, dpp<XOR2>(M)); M = fmaxf(M, dpp<XOR7>(M));
  const float wt = __builtin_amdgcn_exp2f((ml - M) * C2);
  float LL = wt * dpp<XOR8>(ml);
  LL += dpp<XOR1>(LL); LL += dpp<XOR2>(LL); LL += dpp<XOR7>(LL);
  const float inv = 1.0f / lanef(LL, 0);
  f32x2 oo = o * lanef(wt, NSPLIT - 1);
#pragma unroll
  for (int j = 0; j < NSPLIT - 1; ++j) {
    const float wj = lanef(wt, j);
    oo = __builtin_elementwise_fma(f32x2{wj, wj}, f32x2{__uint_as_float(ga[j][0]), __uint_as_float(ga[j][2])}, oo);
  }
  oo *= inv;
  const __hip_bfloat16 b0 = __float2bfloat16(oo[0]), b1 = __float2bfloat16(oo[1]);
  __builtin_amdgcn_raw_buffer_store_b32((unsigned)__builtin_bit_cast(uint16_t, b0) | ((unsigned)__builtin_bit_cast(uint16_t, b1) << 16),
                                        __builtin_amdgcn_make_buffer_rsrc((void*)out, 0, H * D * 2, 0x00020000), lane * 4, h * D * 2, AUX_SC1);
}

extern "C" void kernel_launch(void* const* d_in, const int* in_sizes, int n_in, void* d_out, int out_size, void* d_ws,
                              size_t ws_size, hipStream_t stream) {
  attn_decode<<<H * NSPLIT, NTHR, 0, stream>>>((const uint16_t*)d_in[0], (const uint16_t*)d_in[1], (const uint16_t*)d_in[2],
                                               (__hip_bfloat16*)d_out, (unsigned char*)d_ws);
}
```

```cpp
#define DG 16
#include <hip/hip_runtime.h>
#include <hip/hip_bf16.h>
#include <cstdint>
#include <type_traits>

constexpr unsigned AM[18] = {0x60, 0x28, 0x211, 0x40, 0x1000, 0x139a, 0x1506, 0x81, 0x32d, 0x3, 0x800, 0x324, 0x1340,
                             0x258, 0x0, 0x0, 0x400, 0x110};
constexpr int SLOPE = 2130;
constexpr int THETA = 40;
constexpr int INF = 6;
constexpr int SOFF = 120;
constexpr int NX = 3, X0 = 2, XS = 10, XD = 2;
constexpr int GEXIT = 29, DMAX = 160;

constexpr int H = 32, SK = 32768, D = 128, NSPLIT = 8, WAVES = 4, NTHR = (WAVES + 1) * 64, NGRP = 16;
constexpr uint32_t HEAD_BYTES = (uint32_t)SK * D * 2;
constexpr uint32_t KV_BYTES = (uint32_t)H * HEAD_BYTES;
static_assert(NSPLIT * WAVES * NGRP * 16 * 1024 == HEAD_BYTES, "splits x waves x groups x loads cover a head");
constexpr uint32_t spread(uint32_t x, int first, int n) {
  uint32_t a = 0;
  for (int j = 0; j < n; ++j) if (x >> j & 1) a ^= AM[first + j] << 10;
  return a;
}
constexpr bool map_is_bijective() {
  uint32_t v[13] = {};
  for (int i = 0; i < 13; ++i) v[i] = AM[i];
  for (int i = 0; i < 18; ++i) if (AM[i] >> 13) return false;
  for (int b = 0; b < 13; ++b) {
    int p = -1;
    for (int i = b; i < 13 && p < 0; ++i) if (v[i] >> b & 1) p = i;
    if (p < 0) return false;
    const uint32_t t = v[p]; v[p] = v[b]; v[b] = t;
    for (int i = 0; i < 13; ++i) if (i != b && (v[i] >> b & 1)) v[i] ^= v[b];
  }
  return true;
}
static_assert(map_is_bijective(), "");
constexpr uint32_t WS_EPOCH = 0, WS_ML = 16384, WS_REF = 32768, WS_ACC = 65536, WS_END = WS_ACC + H * NSPLIT * 1024;
constexpr int L_SC = 0, L_ACC = WAVES * NGRP * 64, L_MX = L_ACC + WAVES * D, L_SUM = L_MX + WAVES, L_ORG = L_SUM + WAVES,
              L_GRP = L_ORG + WAVES, L_REF = L_GRP + WAVES, L_END = L_REF + 1;

using bf16x2 = __attribute__((ext_vector_type(2))) __bf16;
using u32x2 = __attribute__((ext_vector_type(2))) unsigned;
using u32x4 = __attribute__((ext_vector_type(4))) unsigned;
using f32x4 = __attribute__((ext_vector_type(4))) float;
using f32x2 = __attribute__((ext_vector_type(2))) float;
using u64 = unsigned long long;
using u64x4 = __attribute__((ext_vector_type(4))) u64;
typedef __attribute__((address_space(1))) u32x4 gu32x4;
typedef __attribute__((address_space(3))) unsigned lu32;
#define RLX_WG __ATOMIC_RELAXED, __HIP_MEMORY_SCOPE_WORKGROUP
constexpr int AUX_NT = 2, AUX_SC1 = 16;

__device__ __forceinline__ float dot2(unsigned k, unsigned q, float acc) {
  return __builtin_amdgcn_fdot2_f32_bf16(__builtin_bit_cast(bf16x2, k), __builtin_bit_cast(bf16x2, q), acc, false);
}
template <int CTRL> __device__ __forceinline__ int dppi(int x) { return __builtin_amdgcn_mov_dpp(x, CTRL, 0xf, 0xf, true); }
template <int CTRL> __device__ __forceinline__ float dpp(float x) { return __builtin_bit_cast(float, dppi<CTRL>(__builtin_bit_cast(int, x))); }
constexpr int XOR1 = 0xB1, XOR2 = 0x4E, XOR7 = 0x141, XOR8 = 0x128, ROWBCAST = 0x150;
__device__ __forceinline__ float fold32(float a, float b) {
  auto t = __builtin_amdgcn_permlane32_swap(__float_as_uint(a), __float_as_uint(b), false, false);
  return __uint_as_float(t[0]) + __uint_as_float(t[1]);
}
__device__ __forceinline__ float fold16(float a, float b) {
  auto t = __builtin_amdgcn_permlane16_swap(__float_as_uint(a), __float_as_uint(b), false, false);
  return __uint_as_float(t[0]) + __uint_as_float(t[1]);
}
__device__ __forceinline__ float wave_max(float x) {
  x = fmaxf(x, dpp<XOR1>(x)); x = fmaxf(x, dpp<XOR2>(x)); x = fmaxf(x, dpp<XOR7>(x)); x = fmaxf(x, dpp<XOR8>(x));
  auto s = __builtin_amdgcn_permlane16_swap(__float_as_uint(x), __float_as_uint(x), false, false);
  x = fmaxf(__uint_as_float(s[0]), __uint_as_float(s[1]));
  auto t = __builtin_amdgcn_permlane32_swap(__float_as_uint(x), __float_as_uint(x), false, false);
  return fmaxf(__uint_as_float(t[0]), __uint_as_float(t[1]));
}
__device__ __forceinline__ float wave_sum(float x) {
  x += dpp<XOR1>(x); x += dpp<XOR2>(x); x += dpp<XOR7>(x); x += dpp<XOR8>(x);
  x = fold16(x, x);
  return fold32(x, x);
}
template <int CTRL, int BIT> __device__ __forceinline__ float tr(float a, float b, int c) {
  const float lo = a + dpp<CTRL>(a), hi = b + dpp<CTRL>(b);
  return (c & BIT) ? hi : lo;
}
__device__ __forceinline__ unsigned uni(unsigned x) { return __builtin_amdgcn_readfirstlane(x); }
__device__ __forceinline__ float lanef(float x, int l) { return __uint_as_float(__builtin_amdgcn_readlane(__float_as_uint(x), l)); }
__device__ __forceinline__ unsigned now() { return (unsigned)__builtin_amdgcn_s_memrealtime(); }
template <int I, int N, class F> __device__ __forceinline__ void sfor(F&& f) {
  if constexpr (I < N) { f(std::integral_constant<int, I>{}); sfor<I + 1, N>(f); }
}
template <int N> using ic = std::integral_constant<int, N>;
__device__ __forceinline__ void wg_barrier() {
  asm volatile("s_waitcnt lgkmcnt(0)\n\ts_barrier" ::: "memory");
}
__device__ __forceinline__ unsigned mix(unsigned e) { return (e ^ 0x5bd1e995u) * 0x9E3779B1u + 0x7f4a7c15u; }
constexpr unsigned PENDING = 0x7fc0dead;

__global__ void __launch_bounds__(NTHR, 2) attn_decode(const uint16_t* q_, const uint16_t* k_, const uint16_t* v_,
                                                       __hip_bfloat16* out_, unsigned char* ws_) {
  __shared__ __attribute__((aligned(16))) float lds[L_END];
  u64 d0; asm volatile("s_memrealtime %0" : "=s"(d0)); __builtin_amdgcn_sched_barrier(0);
  unsigned dq = 0;
  u64x4 a03; u64 a4;
  asm volatile("s_load_dwordx8 %0, %2, 0x0\n\ts_load_dwordx2 %1, %2, 0x20" : "=&s"(a03), "=&s"(a4) : "s"(__builtin_amdgcn_kernarg_segment_ptr()));
  const int h = blockIdx.x / NSPLIT, s = blockIdx.x % NSPLIT;
  const int tid = threadIdx.x, lane = tid & 63, wave = uni(tid >> 6), r = lane >> 4, c = lane & 15;
  uint32_t voff = lane * 16, qoff = h * D * 2 + c * 16;
  uint32_t base = h * HEAD_BYTES ^ spread(wave, 8, 2) ^ spread(s, 10, 3) ^ spread(h, 13, 5);
  asm volatile("" : "+v"(voff), "+v"(qoff));
  asm volatile("" : "+s"(base));
  asm volatile("s_waitcnt lgkmcnt(0)" : "+s"(a03), "+s"(a4));
  const unsigned char* const q = (const unsigned char*)a03[0];
  const void *const k = (const void*)a03[1], *const v = (const void*)a03[2];
  void* const out = (void*)a03[3];
  void* const ws = (void*)a4;
  const auto rw = __builtin_amdgcn_make_buffer_rsrc((void*)ws, 0, WS_END, 0x00020000);
  lu32* const lw = (lu32*)lds;

  if (wave == WAVES) {
    unsigned gep_v = __builtin_amdgcn_raw_buffer_load_b32(rw, 0, WS_EPOCH, AUX_SC1);
    const unsigned nominal = now() + SOFF;
    if (lane < 4 * WAVES + 1)
      __hip_atomic_store(lw + L_MX + lane, lane < WAVES ? PENDING : (lane >= 2 * WAVES && lane < 3 * WAVES) || lane == 4 * WAVES ? nominal : 0u, RLX_WG);
    wg_barrier();
    __builtin_amdgcn_sched_barrier(0);
    asm volatile("" : "+v"(gep_v));
    const unsigned rtag = mix(uni(gep_v)) ^ 0x68e31da4u;
    const int pj = blockIdx.x >> 4;
    const uint32_t pub_off = ((int)(blockIdx.x & 15) == (pj & 7) && lane == 0) ? pj * 8 : WS_END;
    int step = 0;
    int ref = 0, gref = -1, drift = 0;
    for (int it = 0; it < 8192; ++it) {
      __builtin_amdgcn_s_sleep(12);
      const unsigned og = __hip_atomic_load(lw + L_ORG + (lane & 7), RLX_WG);
      int gmin = (int)og; gmin = min(gmin, dppi<XOR1>(gmin)); gmin = min(gmin, dppi<XOR2>(gmin));
      const int G = __builtin_amdgcn_readlane(gmin, 4);
      if (G >= GEXIT) break;
      const int gx = X0 + (step >> 1) * XS;
      if (step < 2 * NX && G >= gx + (step & 1) * XD) {
        asm volatile("" ::: "memory");
        if ((step & 1) == 0) {
          int d = (int)(og - nominal); d += dppi<XOR1>(d); d += dppi<XOR2>(d);
          const unsigned mine = nominal + (unsigned)(__builtin_amdgcn_readlane(d, 0) >> 2);
          __builtin_amdgcn_raw_buffer_store_b64(u32x2{mine, rtag}, rw, pub_off, WS_REF + (step >> 1) * 256, AUX_SC1);
        } else {
          const u32x2 smp = __builtin_amdgcn_raw_buffer_load_b64(rw, c * 8, WS_REF + (step >> 1) * 256, AUX_SC1);
          const bool ok = smp[1] == rtag;
          float e = ok ? (float)(int)(smp[0] - nominal) : 0.f, n = ok ? 1.f : 0.f;
          e += dpp<XOR1>(e); e += dpp<XOR2>(e); e += dpp<XOR7>(e); e += dpp<XOR8>(e);
          n += dpp<XOR1>(n); n += dpp<XOR2>(n); n += dpp<XOR7>(n); n += dpp<XOR8>(n);
          if (lanef(n, 0) >= 8.f) {
            const int mean = (int)uni((unsigned)(int)(e * __builtin_amdgcn_rcpf(n)));
            if (gref >= 0) drift = min(max((mean - ref) * 16 / (gx - gref), -DMAX), DMAX);
            ref = mean; gref = gx;
          }
        }
        ++step;
      }
      if (gref >= 0) __hip_atomic_store(lw + L_REF, nominal + (unsigned)(ref + ((G + 1 - gref) * drift >> 4)), RLX_WG);
    }
    return;
  }

  constexpr float C2 = 0.088388347648318440f * 1.4426950408889634f;
  const auto rk = __builtin_amdgcn_make_buffer_rsrc((void*)k, 0, KV_BYTES, 0x00020000);
  const auto rv = __builtin_amdgcn_make_buffer_rsrc((void*)v, 0, KV_BYTES, 0x00020000);
  u32x4 B[16];
  auto ld = [&](decltype(rk) rs, uint32_t a, auto I) {
    B[I.value] = __builtin_amdgcn_raw_buffer_load_b128(rs, voff, a ^ spread(I.value, 0, 4), AUX_NT);
    __builtin_amdgcn_sched_barrier(0);
  };
  int lag;
  auto steer = [&](int G) {
    const unsigned own = now() - (unsigned)(G * SLOPE) / 10u;
    if (G == DG) dq = own + (unsigned)(G * SLOPE) / 10u - (unsigned)d0;
    lag = (int)(own - uni(__hip_atomic_load(lw + L_REF, RLX_WG))) - THETA;
    if (G > 0) __hip_atomic_store(lw + L_ORG + wave + (lane & 1) * WAVES, (lane & 1) ? (unsigned)G : own, RLX_WG);
  };
  auto arrived = [&](const u32x4& x, auto THROTTLE) {
    asm volatile("" :: "v"(x) : "memory");
    if constexpr (THROTTLE.value)
      asm volatile("s_cmp_gt_i32 %0, 0\n\ts_cbranch_scc1 1f\n\ts_waitcnt vmcnt(%1)\n1:" :: "s"(lag), "i"(INF - 2) : "scc", "memory");
    __builtin_amdgcn_sched_barrier(0);
  };
  const u32x4 qv = *(const gu32x4*)(q + qoff);
  __builtin_amdgcn_sched_barrier(0);
  sfor<0, INF>([&](auto I) { ld(rk, base, I); });
  const unsigned epoch_v = __builtin_amdgcn_raw_buffer_load_b32(rw, 0, WS_EPOCH + h * 256, AUX_SC1);
  asm volatile("s_barrier" ::: "memory");
  float* const sl = lds + L_SC + wave * (NGRP * 64) + lane;
  float mx = -INFINITY;
  auto kgroup = [&](uint32_t cur, decltype(rk) rn, uint32_t next, int g) {
    float x[4];
    steer(g);
    sfor<0, 16>([&](auto I) {
      constexpr int i = I.value;
      arrived(B[i], std::true_type{});
      if constexpr (i + INF < 16) ld(rk, cur, ic<(i + INF) % 16>{});
      else ld(rn, next, ic<(i + INF) % 16>{});
      float d = 0.f;
#pragma unroll
      for (int j = 0; j < 4; ++j) d = dot2(B[i][j], qv[j], d);
      if constexpr ((i & 1) == 1) d = tr<XOR8, 8>(x[0], d, c);
      if constexpr ((i & 3) == 3) d = tr<XOR7, 4>(x[1], d, c);
      if constexpr ((i & 7) == 7) d = tr<XOR2, 2>(x[2], d, c);
      if constexpr (i == 15) d = tr<XOR1, 1>(x[3], d, c);
      asm volatile("" : "+v"(d) :: "memory");
      x[(i & 1) == 0 ? 0 : (i & 3) == 1 ? 1 : (i & 7) == 3 ? 2 : i == 7 ? 3 : 0] = d;
      __builtin_amdgcn_sched_barrier(0);
    });
    mx = fmaxf(mx, x[0]);
    sl[64 * g] = x[0];
    __builtin_amdgcn_sched_barrier(0);
  };
#pragma unroll 1
  for (int g = 0; g < NGRP - 1; ++g) kgroup(base ^ spread(g, 4, 4), rk, base ^ spread(g + 1, 4, 4), g);
  kgroup(base ^ spread(NGRP - 1, 4, 4), rv, base, NGRP - 1);
  const float m = wave_max(mx);
  if (lane == 0) __hip_atomic_store(lw + L_MX + wave, __float_as_uint(m), RLX_WG);
  const unsigned epoch = uni(epoch_v), tag = mix(epoch);
  float mwg, fw;
  float l = 0.f;
  f32x2 acc[4];
#pragma unroll
  for (int i = 0; i < 4; ++i) acc[i] = f32x2{0.f, 0.f};
  auto vgroup = [&](uint32_t cur, uint32_t next, int g, auto LAST) {
    constexpr bool last = LAST.value;
    steer(NGRP + g);
    const float p = __builtin_amdgcn_exp2f((sl[64 * g] - m) * C2);
    l += p;
    sfor<0, 16>([&](auto I) {
      constexpr int i = I.value;
      arrived(B[i], std::integral_constant<bool, !last || i + INF < 16>{});
      if constexpr (i + INF < 16) ld(rv, cur, ic<(i + INF) % 16>{});
      else if constexpr (!last) ld(rv, next, ic<(i + INF) % 16>{});
      if constexpr (last && i == 15 - INF) {
        unsigned u[WAVES];
        for (int spin = 0; spin < (1 << 20); ++spin) {
#pragma unroll
          for (int w = 0; w < WAVES; ++w) u[w] = __hip_atomic_load(lw + L_MX + w, RLX_WG);
          if (uni((u[0] == PENDING) | (u[1] == PENDING) | (u[2] == PENDING) | (u[3] == PENDING)) == 0) break;
        }
        mwg = fmaxf(fmaxf(__uint_as_float(u[0]), __uint_as_float(u[1])), fmaxf(__uint_as_float(u[2]), __uint_as_float(u[3])));
        fw = __builtin_amdgcn_exp2f((m - mwg) * C2);
        l = wave_sum(l) * fw;
        if (lane == 0) lds[L_SUM + wave] = l;
        __builtin_amdgcn_sched_barrier(0);
      }
      const float pb = dpp<ROWBCAST + ((i & 1) << 3 | (i & 2) << 1 | (i & 4) >> 1 | (i & 8) >> 3)>(p);
#pragma unroll
      for (int j = 0; j < 4; ++j) {
        const f32x2 vv = {__builtin_bit_cast(float, B[i][j] << 16), __builtin_bit_cast(float, B[i][j] & 0xffff0000u)};
        acc[j] = __builtin_elementwise_fma(f32x2{pb, pb}, vv, acc[j]);
      }
#pragma unroll
      for (int j = 0; j < 4; ++j) asm volatile("" : "+v"(acc[j]) :: "memory");
      __builtin_amdgcn_sched_barrier(0);
    });
  };
#pragma unroll 1
  for (int g = 0; g < NGRP - 1; ++g) vgroup(base ^ spread(g, 4, 4), base ^ spread(g + 1, 4, 4), g, std::false_type{});
  vgroup(base ^ spread(NGRP - 1, 4, 4), 0, NGRP - 1, std::true_type{});
  if (DG == 32) dq = now() - (unsigned)d0;
  {
    const float y0 = fold32(acc[0][0], acc[2][0]), y1 = fold32(acc[0][1], acc[2][1]);
    const float y2 = fold32(acc[1][0], acc[3][0]), y3 = fold32(acc[1][1], acc[3][1]);
    *reinterpret_cast<f32x2*>(lds + L_ACC + wave * D + 8 * c + 2 * r) = f32x2{fold16(y0, y2), fold16(y1, y3)} * fw;
  }
  const bool merger = s == NSPLIT - 1;
  u32x4 ga[NSPLIT - 1];
  u32x2 gm;
  auto poll = [&]() {
    asm volatile("" ::: "memory");
    gm = __builtin_amdgcn_raw_buffer_load_b64(rw, c * 8, WS_ML + h * 128, AUX_SC1);
#pragma unroll
    for (int j = 0; j < NSPLIT - 1; ++j)
      ga[j] = __builtin_amdgcn_raw_buffer_load_b128(rw, voff + (j & 3) * 1024, WS_ACC + h * (NSPLIT * 1024) + (j >> 2) * 4096, AUX_SC1);
    asm volatile("" ::: "memory");
    __builtin_amdgcn_sched_barrier(0);
  };
  if (merger && wave == 0) poll();
  wg_barrier();
  if (wave != 0) { const unsigned tend = (unsigned)d0 + 10000 + dq; for (int i = 0; i < 1000000 && (int)(now() - tend) < 0; ++i) __builtin_amdgcn_s_sleep(1); return; }
  f32x2 o = *reinterpret_cast<const f32x2*>(lds + L_ACC + 2 * lane);
#pragma unroll
  for (int w = 1; w < WAVES; ++w) o += *reinterpret_cast<const f32x2*>(lds + L_ACC + w * D + 2 * lane);
  const f32x4 l4 = *reinterpret_cast<const f32x4*>(lds + L_SUM);
  const float L = (l4[0] + l4[1]) + (l4[2] + l4[3]);
  if (!merger) {
    __builtin_amdgcn_raw_buffer_store_b128(u32x4{__float_as_uint(o[0]), tag, __float_as_uint(o[1]), tag}, rw, voff, WS_ACC + blockIdx.x * 1024, AUX_SC1);
    __builtin_amdgcn_raw_buffer_store_b64(u32x2{__float_as_uint(lane ? L : mwg), tag}, rw, lane < 2 ? lane * 64u : WS_END, WS_ML + h * 128 + s * 8, AUX_SC1);
    return;
  }
  for (int spin = 0; spin < (1 << 21); ++spin) {
    unsigned lo = (lane & 7) == NSPLIT - 1 ? tag : gm[1], hi = lo;
#pragma unroll
    for (int j = 0; j < NSPLIT - 1; ++j) { lo = min(lo, min(ga[j][1], ga[j][3])); hi = max(hi, max(ga[j][1], ga[j][3])); }
    if (__all(lo == tag && hi == tag)) break;
    poll();
  }
  __builtin_amdgcn_raw_buffer_store_b32(epoch + 1u, rw, lane == 0 ? 0u : WS_END, WS_EPOCH + h * 256, AUX_SC1);
  __builtin_amdgcn_sched_barrier(0);
  const float ml = (lane & 7) == NSPLIT - 1 ? ((lane & 8) ? L : mwg) : __uint_as_float(gm[0]);
  float M = ml;
  M = fmaxf(M, dpp<XOR1>(M)); M = fmaxf(M, dpp<XOR2>(M)); M = fmaxf(M, dpp<XOR7>(M));
  const float wt = __builtin_amdgcn_exp2f((ml - M) * C2);
  float LL = wt * dpp<XOR8>(ml);
  LL += dpp<XOR1>(LL); LL += dpp<XOR2>(LL); LL += dpp<XOR7>(LL);
  const float inv = 1.0f / lanef(LL, 0);
  f32x2 oo = o * lanef(wt, NSPLIT - 1);
#pragma unroll
  for (int j = 0; j < NSPLIT - 1; ++j) {
    const float wj = lanef(wt, j);
    oo = __builtin_elementwise_fma(f32x2{wj, wj}, f32x2{__uint_as_float(ga[j][0]), __uint_as_float(ga[j][2])}, oo);
  }
  oo *= inv;
  const __hip_bfloat16 b0 = __float2bfloat16(oo[0]), b1 = __float2bfloat16(oo[1]);
  __builtin_amdgcn_raw_buffer_store_b32((unsigned)__builtin_bit_cast(uint16_t, b0) | ((unsigned)__builtin_bit_cast(uint16_t, b1) << 16),
                                        __builtin_amdgcn_make_buffer_rsrc((void*)out, 0, H * D * 2, 0x00020000), lane * 4, h * D * 2, AUX_SC1);
}

extern "C" void kernel_launch(void* const* d_in, const int* in_sizes, int n_in, void* d_out, int out_size, void* d_ws,
                              size_t ws_size, hipStream_t stream) {
  attn_decode<<<H * NSPLIT, NTHR, 0, stream>>>((const uint16_t*)d_in[0], (const uint16_t*)d_in[1], (const uint16_t*)d_in[2],
                                               (__hip_bfloat16*)d_out, (unsigned char*)d_ws);
}
```

```cpp
#include <hip/hip_runtime.h>
#include <hip/hip_bf16.h>
#include <cstdint>
#include <type_traits>

constexpr unsigned AM[18] = {0x60, 0x28, 0x211, 0x40, 0x1000, 0x139a, 0x1506, 0x81, 0x32d, 0x3, 0x800, 0x324, 0x1340,
                             0x258, 0x0, 0x0, 0x400, 0x110};
constexpr int SLOPE = 2130;
constexpr int THETA = 40;
constexpr int INF = 6;
constexpr int SOFF = 120;
constexpr int NX = 3, X0 = 2, XS = 10, XD = 2;
constexpr int GEXIT = 29, DMAX = 160;

constexpr int H = 32, SK = 32768, D = 128, NSPLIT = 8, WAVES = 4, NTHR = (WAVES + 1) * 64, NGRP = 16;
constexpr uint32_t HEAD_BYTES = (uint32_t)SK * D * 2;
constexpr uint32_t KV_BYTES = (uint32_t)H * HEAD_BYTES;
static_assert(NSPLIT * WAVES * NGRP * 16 * 1024 == HEAD_BYTES, "splits x waves x groups x loads cover a head");
constexpr uint32_t spread(uint32_t x, int first, int n) {
  uint32_t a = 0;
  for (int j = 0; j < n; ++j) if (x >> j & 1) a ^= AM[first + j] << 10;
  return a;
}
constexpr bool map_is_bijective() {
  uint32_t v[13] = {};
  for (int i = 0; i < 13; ++i) v[i] = AM[i];
  for (int i = 0; i < 18; ++i) if (AM[i] >> 13) return false;
  for (int b = 0; b < 13; ++b) {
    int p = -1;
    for (int i = b; i < 13 && p < 0; ++i) if (v[i] >> b & 1) p = i;
    if (p < 0) return false;
    const uint32_t t = v[p]; v[p] = v[b]; v[b] = t;
    for (int i = 0; i < 13; ++i) if (i != b && (v[i] >> b & 1)) v[i] ^= v[b];
  }
  return true;
}
static_assert(map_is_bijective(), "");
constexpr uint32_t WS_EPOCH = 0, WS_ML = 16384, WS_REF = 32768, WS_ACC = 65536, WS_END = WS_ACC + H * NSPLIT * 1024;
constexpr int L_SC = 0, L_ACC = WAVES * NGRP * 64, L_MX = L_ACC + WAVES * D, L_SUM = L_MX + WAVES, L_ORG = L_SUM + WAVES,
              L_GRP = L_ORG + WAVES, L_REF = L_GRP + WAVES, L_END = L_REF + 1;

using bf16x2 = __attribute__((ext_vector_type(2))) __bf16;
using u32x2 = __attribute__((ext_vector_type(2))) unsigned;
using u32x4 = __attribute__((ext_vector_type(4))) unsigned;
using f32x4 = __attribute__((ext_vector_type(4))) float;
using f32x2 = __attribute__((ext_vector_type(2))) float;
using u64 = unsigned long long;
using u64x4 = __attribute__((ext_vector_type(4))) u64;
typedef __attribute__((address_space(1))) u32x4 gu32x4;
typedef __attribute__((address_space(3))) unsigned lu32;
#define RLX_WG __ATOMIC_RELAXED, __HIP_MEMORY_SCOPE_WORKGROUP
constexpr int AUX_NT = 2, AUX_SC1 = 16;

__device__ __forceinline__ float dot2(unsigned k, unsigned q, float acc) {
  return __builtin_amdgcn_fdot2_f32_bf16(__builtin_bit_cast(bf16x2, k), __builtin_bit_cast(bf16x2, q), acc, false);
}
template <int CTRL> __device__ __forceinline__ int dppi(int x) { return __builtin_amdgcn_mov_dpp(x, CTRL, 0xf, 0xf, true); }
template <int CTRL> __device__ __forceinline__ float dpp(float x) { return __builtin_bit_cast(float, dppi<CTRL>(__builtin_bit_cast(int, x))); }
constexpr int XOR1 = 0xB1, XOR2 = 0x4E, XOR7 = 0x141, XOR8 = 0x128, ROWBCAST = 0x150;
__device__ __forceinline__ float fold32(float a, float b) {
  auto t = __builtin_amdgcn_permlane32_swap(__float_as_uint(a), __float_as_uint(b), false, false);
  return __uint_as_float(t[0]) + __uint_as_float(t[1]);
}
__device__ __forceinline__ float fold16(float a, float b) {
  auto t = __builtin_amdgcn_permlane16_swap(__float_as_uint(a), __float_as_uint(b), false, false);
  return __uint_as_float(t[0]) + __uint_as_float(t[1]);
}
__device__ __forceinline__ float wave_max(float x) {
  x = fmaxf(x, dpp<XOR1>(x)); x = fmaxf(x, dpp<XOR2>(x)); x = fmaxf(x, dpp<XOR7>(x)); x = fmaxf(x, dpp<XOR8>(x));
  auto s = __builtin_amdgcn_permlane16_swap(__float_as_uint(x), __float_as_uint(x), false, false);
  x = fmaxf(__uint_as_float(s[0]), __uint_as_float(s[1]));
  auto t = __builtin_amdgcn_permlane32_swap(__float_as_uint(x), __float_as_uint(x), false, false);
  return fmaxf(__uint_as_float(t[0]), __uint_as_float(t[1]));
}
__device__ __forceinline__ float wave_sum(float x) {
  x += dpp<XOR1>(x); x += dpp<XOR2>(x); x += dpp<XOR7>(x); x += dpp<XOR8>(x);
  x = fold16(x, x);
  return fold32(x, x);
}
template <int CTRL, int BIT> __device__ __forceinline__ float tr(float a, float b, int c) {
  const float lo = a + dpp<CTRL>(a), hi = b + dpp<CTRL>(b);
  return (c & BIT) ? hi : lo;
}
__device__ __forceinline__ unsigned uni(unsigned x) { return __builtin_amdgcn_readfirstlane(x); }
__device__ __forceinline__ float lanef(float x, int l) { return __uint_as_float(__builtin_amdgcn_readlane(__float_as_uint(x), l)); }
__device__ __forceinline__ unsigned now() { return (unsigned)__builtin_amdgcn_s_memrealtime(); }
template <int I, int N, class F> __device__ __forceinline__ void sfor(F&& f) {
  if constexpr (I < N) { f(std::integral_constant<int, I>{}); sfor<I + 1, N>(f); }
}
template <int N> using ic = std::integral_constant<int, N>;
__device__ __forceinline__ void wg_barrier() {
  asm volatile("s_waitcnt lgkmcnt(0)\n\ts_barrier" ::: "memory");
}
__device__ __forceinline__ unsigned mix(unsigned e) { return (e ^ 0x5bd1e995u) * 0x9E3779B1u + 0x7f4a7c15u; }
constexpr unsigned PENDING = 0x7fc0dead;

__global__ void __launch_bounds__(NTHR, 2) attn_decode(const uint16_t* q_, const uint16_t* k_, const uint16_t* v_,
                                                       __hip_bfloat16* out_, unsigned char* ws_) {
  __shared__ __attribute__((aligned(16))) float lds[L_END];
  u64x4 a03; u64 a4;
  asm volatile("s_load_dwordx8 %0, %2, 0x0\n\ts_load_dwordx2 %1, %2, 0x20" : "=&s"(a03), "=&s"(a4) : "s"(__builtin_amdgcn_kernarg_segment_ptr()));
  const int h = blockIdx.x / NSPLIT, s = blockIdx.x % NSPLIT;
  const int tid = threadIdx.x, lane = tid & 63, wave = uni(tid >> 6), r = lane >> 4, c = lane & 15;
  uint32_t voff = lane * 16, qoff = h * D * 2 + c * 16;
  uint32_t base = h * HEAD_BYTES ^ spread(wave, 8, 2) ^ spread(s, 10, 3) ^ spread(h, 13, 5);
  asm volatile("" : "+v"(voff), "+v"(qoff));
  asm volatile("" : "+s"(base));
  asm volatile("s_waitcnt lgkmcnt(0)" : "+s"(a03), "+s"(a4));
  const unsigned char* const q = (const unsigned char*)a03[0];
  const void *const k = (const void*)a03[1], *const v = (const void*)a03[2];
  void* const out = (void*)a03[3];
  void* const ws = (void*)a4;
  const auto rw = __builtin_amdgcn_make_buffer_rsrc((void*)ws, 0, WS_END, 0x00020000);
  lu32* const lw = (lu32*)lds;

  if (wave == WAVES) {
    unsigned gep_v = __builtin_amdgcn_raw_buffer_load_b32(rw, 0, WS_EPOCH, AUX_SC1);
    const unsigned nominal = now() + SOFF;
    if (lane < 4 * WAVES + 1)
      __hip_atomic_store(lw + L_MX + lane, lane < WAVES ? PENDING : (lane >= 2 * WAVES && lane < 3 * WAVES) || lane == 4 * WAVES ? nominal : 0u, RLX_WG);
    wg_barrier();
    __builtin_amdgcn_sched_barrier(0);
    asm volatile("" : "+v"(gep_v));
    const unsigned rtag = mix(uni(gep_v)) ^ 0x68e31da4u;
    const int pj = blockIdx.x >> 4;
    const uint32_t pub_off = ((int)(blockIdx.x & 15) == (pj & 7) && lane == 0) ? pj * 8 : WS_END;
    int step = 0;
    int ref = 0, gref = -1, drift = 0;
    for (int it = 0; it < 8192; ++it) {
      __builtin_amdgcn_s_sleep(12);
      const unsigned og = __hip_atomic_load(lw + L_ORG + (lane & 7), RLX_WG);
      int gmin = (int)og; gmin = min(gmin, dppi<XOR1>(gmin)); gmin = min(gmin, dppi<XOR2>(gmin));
      const int G = __builtin_amdgcn_readlane(gmin, 4);
      if (G >= GEXIT) break;
      const int gx = X0 + (step >> 1) * XS;
      if (step < 2 * NX && G >= gx + (step & 1) * XD) {
        asm volatile("" ::: "memory");
        if ((step & 1) == 0) {
          int d = (int)(og - nominal); d += dppi<XOR1>(d); d += dppi<XOR2>(d);
          const unsigned mine = nominal + (unsigned)(__builtin_amdgcn_readlane(d, 0) >> 2);
          __builtin_amdgcn_raw_buffer_store_b64(u32x2{mine, rtag}, rw, pub_off, WS_REF + (step >> 1) * 256, AUX_SC1);
        } else {
          const u32x2 smp = __builtin_amdgcn_raw_buffer_load_b64(rw, c * 8, WS_REF + (step >> 1) * 256, AUX_SC1);
          const bool ok = smp[1] == rtag;
          float e = ok ? (float)(int)(smp[0] - nominal) : 0.f, n = ok ? 1.f : 0.f;
          e += dpp<XOR1>(e); e += dpp<XOR2>(e); e += dpp<XOR7>(e); e += dpp<XOR8>(e);
          n += dpp<XOR1>(n); n += dpp<XOR2>(n); n += dpp<XOR7>(n); n += dpp<XOR8>(n);
          if (lanef(n, 0) >= 8.f) {
            const int mean = (int)uni((unsigned)(int)(e * __builtin_amdgcn_rcpf(n)));
            if (gref >= 0) drift = min(max((mean - ref) * 16 / (gx - gref), -DMAX), DMAX);
            ref = mean; gref = gx;
          }
        }
        ++step;
      }
      if (gref >= 0) __hip_atomic_store(lw + L_REF, nominal + (unsigned)(ref + ((G + 1 - gref) * drift >> 4)), RLX_WG);
    }
    return;
  }

  constexpr float C2 = 0.088388347648318440f * 1.4426950408889634f;
  const auto rk = __builtin_amdgcn_make_buffer_rsrc((void*)k, 0, KV_BYTES, 0x00020000);
  const auto rv = __builtin_amdgcn_make_buffer_rsrc((void*)v, 0, KV_BYTES, 0x00020000);
  u32x4 B[16];
  auto ld = [&](decltype(rk) rs, uint32_t a, auto I) {
    B[I.value] = __builtin_amdgcn_raw_buffer_load_b128(rs, voff, a ^ spread(I.value, 0, 4), AUX_NT);
    __builtin_amdgcn_sched_barrier(0);
  };
  int lag;
  auto steer = [&](int G) {
    const unsigned own = now() - (unsigned)(G * SLOPE) / 10u;
    lag = (int)(own - uni(__hip_atomic_load(lw + L_REF, RLX_WG))) - THETA;
    if (G > 0) __hip_atomic_store(lw + L_ORG + wave + (lane & 1) * WAVES, (lane & 1) ? (unsigned)G : own, RLX_WG);
  };
  auto arrived = [&](const u32x4& x, auto THROTTLE) {
    asm volatile("" :: "v"(x) : "memory");
    if constexpr (THROTTLE.value)
      asm volatile("s_cmp_gt_i32 %0, 0\n\ts_cbranch_scc1 1f\n\ts_waitcnt vmcnt(%1)\n1:" :: "s"(lag), "i"(INF - 2) : "scc", "memory");
    __builtin_amdgcn_sched_barrier(0);
  };
  const u32x4 qv = *(const gu32x4*)(q + qoff);
  __builtin_amdgcn_sched_barrier(0);
  sfor<0, INF>([&](auto I) { ld(rk, base, I); });
  const unsigned epoch_v = __builtin_amdgcn_raw_buffer_load_b32(rw, 0, WS_EPOCH + h * 256, AUX_SC1);
  asm volatile("s_barrier" ::: "memory");
  float* const sl = lds + L_SC + wave * (NGRP * 64) + lane;
  float mx = -INFINITY;
  auto kgroup = [&](uint32_t cur, decltype(rk) rn, uint32_t next, int g) {
    float x[4];
    steer(g);
    sfor<0, 16>([&](auto I) {
      constexpr int i = I.value;
      arrived(B[i], std::true_type{});
      if constexpr (i + INF < 16) ld(rk, cur, ic<(i + INF) % 16>{});
      else ld(rn, next, ic<(i + INF) % 16>{});
      float d = 0.f;
#pragma unroll
      for (int j = 0; j < 4; ++j) d = dot2(B[i][j], qv[j], d);
      if constexpr ((i & 1) == 1) d = tr<XOR8, 8>(x[0], d, c);
      if constexpr ((i & 3) == 3) d = tr<XOR7, 4>(x[1], d, c);
      if constexpr ((i & 7) == 7) d = tr<XOR2, 2>(x[2], d, c);
      if constexpr (i == 15) d = tr<XOR1, 1>(x[3], d, c);
      asm volatile("" : "+v"(d) :: "memory");
      x[(i & 1) == 0 ? 0 : (i & 3) == 1 ? 1 : (i & 7) == 3 ? 2 : i == 7 ? 3 : 0] = d;
      __builtin_amdgcn_sched_barrier(0);
    });
    mx = fmaxf(mx, x[0]);
    sl[64 * g] = x[0];
    __builtin_amdgcn_sched_barrier(0);
  };
#pragma unroll 1
  for (int g = 0; g < NGRP - 1; ++g) kgroup(base ^ spread(g, 4, 4), rk, base ^ spread(g + 1, 4, 4), g);
  kgroup(base ^ spread(NGRP - 1, 4, 4), rv, base, NGRP - 1);
  const float m = wave_max(mx);
  if (lane == 0) __hip_atomic_store(lw + L_MX + wave, __float_as_uint(m), RLX_WG);
  const unsigned epoch = uni(epoch_v), tag = mix(epoch);
  float mwg, fw;
  float l = 0.f;
  f32x2 acc[4];
#pragma unroll
  for (int i = 0; i < 4; ++i) acc[i] = f32x2{0.f, 0.f};
  auto vgroup = [&](uint32_t cur, uint32_t next, int g, auto LAST) {
    constexpr bool last = LAST.value;
    steer(NGRP + g);
    const float p = __builtin_amdgcn_exp2f((sl[64 * g] - m) * C2);
    l += p;
    sfor<0, 16>([&](auto I) {
      constexpr int i = I.value;
      arrived(B[i], std::integral_constant<bool, !last || i + INF < 16>{});
      if constexpr (i + INF < 16) ld(rv, cur, ic<(i + INF) % 16>{});
      else if constexpr (!last) ld(rv, next, ic<(i + INF) % 16>{});
      if constexpr (last && i == 15 - INF) {
        unsigned u[WAVES];
        for (int spin = 0; spin < (1 << 20); ++spin) {
#pragma unroll
          for (int w = 0; w < WAVES; ++w) u[w] = __hip_atomic_load(lw + L_MX + w, RLX_WG);
          if (uni((u[0] == PENDING) | (u[1] == PENDING) | (u[2] == PENDING) | (u[3] == PENDING)) == 0) break;
        }
        mwg = fmaxf(fmaxf(__uint_as_float(u[0]), __uint_as_float(u[1])), fmaxf(__uint_as_float(u[2]), __uint_as_float(u[3])));
        fw = __builtin_amdgcn_exp2f((m - mwg) * C2);
        l = wave_sum(l) * fw;
        if (lane == 0) lds[L_SUM + wave] = l;
        __builtin_amdgcn_sched_barrier(0);
      }
      const float pb = dpp<ROWBCAST + ((i & 1) << 3 | (i & 2) << 1 | (i & 4) >> 1 | (i & 8) >> 3)>(p);
#pragma unroll
      for (int j = 0; j < 4; ++j) {
        const f32x2 vv = {__builtin_bit_cast(float, B[i][j] << 16), __builtin_bit_cast(float, B[i][j] & 0xffff0000u)};
        acc[j] = __builtin_elementwise_fma(f32x2{pb, pb}, vv, acc[j]);
      }
#pragma unroll
      for (int j = 0; j < 4; ++j) asm volatile("" : "+v"(acc[j]) :: "memory");
      __builtin_amdgcn_sched_barrier(0);
    });
  };
#pragma unroll 1
  for (int g = 0; g < NGRP - 1; ++g) vgroup(base ^ spread(g, 4, 4), base ^ spread(g + 1, 4, 4), g, std::false_type{});
  vgroup(base ^ spread(NGRP - 1, 4, 4), 0, NGRP - 1, std::true_type{});
  {
    const float y0 = fold32(acc[0][0], acc[2][0]), y1 = fold32(acc[0][1], acc[2][1]);
    const float y2 = fold32(acc[1][0], acc[3][0]), y3 = fold32(acc[1][1], acc[3][1]);
    *reinterpret_cast<f32x2*>(lds + L_ACC + wave * D + 8 * c + 2 * r) = f32x2{fold16(y0, y2), fold16(y1, y3)} * fw;
  }
  const bool merger = s == NSPLIT - 1;
  u32x4 ga[NSPLIT - 1];
  u32x2 gm;
  auto poll = [&]() {
    asm volatile("" ::: "memory");
    gm = __builtin_amdgcn_raw_buffer_load_b64(rw, c * 8, WS_ML + h * 128, AUX_SC1);
#pragma unroll
    for (int j = 0; j < NSPLIT - 1; ++j)
      ga[j] = __builtin_amdgcn_raw_buffer_load_b128(rw, voff + (j & 3) * 1024, WS_ACC + h * (NSPLIT * 1024) + (j >> 2) * 4096, AUX_SC1);
    asm volatile("" ::: "memory");
    __builtin_amdgcn_sched_barrier(0);
  };
  if (merger && wave == 0) poll();
  wg_barrier();
  if (wave != 0) return;
  f32x2 o = *reinterpret_cast<const f32x2*>(lds + L_ACC + 2 * lane);
#pragma unroll
  for (int w = 1; w < WAVES; ++w) o += *reinterpret_cast<const f32x2*>(lds + L_ACC + w * D + 2 * lane);
  const f32x4 l4 = *reinterpret_cast<const f32x4*>(lds + L_SUM);
  const float L = (l4[0] + l4[1]) + (l4[2] + l4[3]);
  if (!merger) {
    __builtin_amdgcn_raw_buffer_store_b128(u32x4{__float_as_uint(o[0]), tag, __float_as_uint(o[1]), tag}, rw, voff, WS_ACC + blockIdx.x * 1024, AUX_SC1);
    __builtin_amdgcn_raw_buffer_store_b64(u32x2{__float_as_uint(lane ? L : mwg), tag}, rw, lane < 2 ? lane * 64u : WS_END, WS_ML + h * 128 + s * 8, AUX_SC1);
    return;
  }
  for (int spin = 0; spin < (1 << 21); ++spin) {
    unsigned lo = (lane & 7) == NSPLIT - 1 ? tag : gm[1], hi = lo;
#pragma unroll
    for (int j = 0; j < NSPLIT - 1; ++j) { lo = min(lo, min(ga[j][1], ga[j][3])); hi = max(hi, max(ga[j][1], ga[j][3])); }
    if (__all(lo == tag && hi == tag)) break;
    poll();
  }
  __builtin_amdgcn_raw_buffer_store_b32(epoch + 1u, rw, lane == 0 ? 0u : WS_END, WS_EPOCH + h * 256, AUX_SC1);
  __builtin_amdgcn_sched_barrier(0);
  const float ml = (lane & 7) == NSPLIT - 1 ? ((lane & 8) ? L : mwg) : __uint_as_float(gm[0]);
  float M = ml;
  M = fmaxf(M, dpp<XOR1>(M)); M = fmaxf(M, dpp<XOR2>(M)); M = fmaxf(M, dpp<XOR7>(M));
  const float wt = __builtin_amdgcn_exp2f((ml - M) * C2);
  float LL = wt * dpp<XOR8>(ml);
  LL += dpp<XOR1>(LL); LL += dpp<XOR2>(LL); LL += dpp<XOR7>(LL);
  const float inv = 1.0f / lanef(LL, 0);
  f32x2 oo = o * lanef(wt, NSPLIT - 1);
#pragma unroll
  for (int j = 0; j < NSPLIT - 1; ++j) {
    const float wj = lanef(wt, j);
    oo = __builtin_elementwise_fma(f32x2{wj, wj}, f32x2{__uint_as_float(ga[j][0]), __uint_as_float(ga[j][2])}, oo);
  }
  oo *= inv;
  const __hip_bfloat16 b0 = __float2bfloat16(oo[0]), b1 = __float2bfloat16(oo[1]);
  __builtin_amdgcn_raw_buffer_store_b32((unsigned)__builtin_bit_cast(uint16_t, b0) | ((unsigned)__builtin_bit_cast(uint16_t, b1) << 16),
                                        __builtin_amdgcn_make_buffer_rsrc((void*)out, 0, H * D * 2, 0x00020000), lane * 4, h * D * 2, AUX_SC1);
}

extern "C" void kernel_launch(void* const* d_in, const int* in_sizes, int n_in, void* d_out, int out_size, void* d_ws,
                              size_t ws_size, hipStream_t stream) {
  attn_decode<<<H * NSPLIT, NTHR, 0, stream>>>((const uint16_t*)d_in[0], (const uint16_t*)d_in[1], (const uint16_t*)d_in[2],
                                               (__hip_bfloat16*)d_out, (unsigned char*)d_ws);
}
```
